```python
import torch

def _softmax(x, axis=-1):
    return torch.softmax(x, dim=axis)

def _gelu(x):
    return torch.nn.functional.gelu(x, approximate='tanh')

def _silu(x):
    return torch.nn.functional.silu(x)

def _sigmoid(x):
    return torch.sigmoid(x)

def _layernorm(x, axis=-1, eps=1e-05):
    m = x.mean(dim=axis, keepdim=True)
    v = x.var(dim=axis, keepdim=True, unbiased=False)
    return (x - m) / torch.sqrt(v + eps)

def _scatter_add(tgt, idx, src, axis=0):
    return tgt.clone().index_add_(axis, idx, src)

def _segment_sum(src, seg, n):
    out = torch.zeros((n,) + src.shape[1:], dtype=src.dtype)
    return out.index_add_(0, seg, src)

def _pad(x, pw):
    flat = tuple((v for pair in reversed(pw) for v in pair))
    return torch.nn.functional.pad(x, flat)
kernel_name = 'interleave_layers__ssm_conv_h3__mixer_gmlp__prompt'

def setup_inputs():

    _w = lambda a: (lambda t: t.to(torch.bfloat16) if t.is_floating_point() else t)(torch.as_tensor(a))

    def _A_setup():
        B, S, D = (1, 2048, 640)
        ks = 4
        return {'q': _w((lambda *s: torch.randn(*s, dtype=torch.bfloat16) * 0.05)(B, S, D)), 'k': _w((lambda *s: torch.randn(*s, dtype=torch.bfloat16) * 1.6)(B, S, D)), 'v': _w((lambda *s: torch.randn(*s, dtype=torch.bfloat16) * 1.6)(B, S, D)), 'shift_k': _w((lambda *s: torch.randn(*s, dtype=torch.bfloat16) * 1.6)(D, ks) * 0.5), 'ssm_k': _w((lambda *s: torch.randn(*s, dtype=torch.bfloat16) * 1.6)(D, S) * (1.0 / S ** 0.5)), 'ssm_D': _w((lambda *s: torch.randn(*s, dtype=torch.bfloat16) * 1.6)(D))}
    _A_in = _A_setup()

    def _B_setup():
        B, S, D, F = (1, 2048, 640, 1728)
        Fh = F // 2
        return {'x': _w((lambda *s: torch.randn(*s, dtype=torch.bfloat16) * 0.05)(B, S, D)), 'w_in': _w((lambda *s: torch.randn(*s, dtype=torch.bfloat16) * 1.6)(D, 2 * Fh)), 'w_sp': _w(0.01 * (lambda *s: torch.randn(*s, dtype=torch.bfloat16) * 1.6)(S, S)), 'b_sp': _w(1.0 + 0.01 * (lambda *s: torch.randn(*s, dtype=torch.bfloat16) * 0.2)(S)), 'w_out': _w((lambda *s: torch.randn(*s, dtype=torch.bfloat16) * 0.2)(Fh, D))}
    _B_in = _B_setup()
    return {'A_q': _A_in['q'], 'A_k': _A_in['k'], 'A_v': _A_in['v'], 'A_shift_k': _A_in['shift_k'], 'A_ssm_k': _A_in['ssm_k'], 'A_ssm_D': _A_in['ssm_D'], 'B_x': _B_in['x'], 'B_w_in': _B_in['w_in'], 'B_w_sp': _B_in['w_sp'], 'B_b_sp': _B_in['b_sp'], 'B_w_out': _B_in['w_out']}

def reference(A_q, A_k, A_v, A_shift_k, A_ssm_k, A_ssm_D, B_x, B_w_in, B_w_sp, B_b_sp, B_w_out):

    def _A_ref(q, k, v, shift_k, ssm_k, ssm_D):
        B, S, D = q.shape
        ks = shift_k.shape[-1]
        k_pad = _pad(k, ((0, 0), (ks - 1, 0), (0, 0)))
        k_shift = torch.zeros_like(k)
        for t in range(ks):
            k_shift = k_shift + k_pad[:, t:t + S, :] * shift_k[:, ks - 1 - t][None, None, :]
        kv = k_shift * v
        L = 2 * S
        kv_pad = _pad(kv, ((0, 0), (0, S), (0, 0)))
        kf_pad = _pad(ssm_k, ((0, 0), (0, S)))
        kv_f = (lambda x, axes=(-2, -1): torch.fft.rfft2(x.float(), dim=axes))(kv_pad[:, None, :, :], axes=(1, 2))
        kf_f = (lambda x, axes=(-2, -1): torch.fft.rfft2(x.float(), dim=axes))(kf_pad[:, None, :], axes=(1, 2))
        kf_f = (lambda x, *d: x.permute(*d))(kf_f, 1, 2, 0)
        y_f = kv_f * kf_f[None, :, :, :]
        y = (lambda x, s, axes=(-2, -1): torch.fft.irfft2(x, s=s, dim=axes).to(torch.bfloat16))(y_f, (1, L), axes=(1, 2))[:, 0, :S, :]
        y = y + kv * ssm_D[None, None, :]
        return q * y

    def _B_ref(x, w_in, w_sp, b_sp, w_out):
        B, S, D = x.shape
        F = w_in.shape[-1]
        Fh = F // 2
        z = _gelu(torch.einsum('bsd,df->bsf', x, w_in))
        z1 = z[..., :Fh].clone()
        z2 = _layernorm(z[..., Fh:], axis=-1)
        g = torch.einsum('ts,bsf->btf', w_sp, z2) + b_sp[None, :, None]
        return torch.einsum('bsf,fd->bsd', z1 * g, w_out)
    x = A_q
    x = x + _A_ref(_layernorm(x, axis=-1), A_k, A_v, A_shift_k, A_ssm_k, A_ssm_D)
    x = x + _B_ref(_layernorm(x, axis=-1), B_w_in, B_w_sp, B_b_sp, B_w_out)
    x = x + _A_ref(_layernorm(x, axis=-1), A_k, A_v, A_shift_k, A_ssm_k, A_ssm_D)
    x = x + _B_ref(_layernorm(x, axis=-1), B_w_in, B_w_sp, B_b_sp, B_w_out)
    return x
```

```cpp
#include <hip/hip_runtime.h>
#include <hip/hip_bf16.h>
#include <stdint.h>

typedef unsigned short u16;
typedef unsigned int u32;
typedef __attribute__((ext_vector_type(8))) short bf16x8;
typedef __attribute__((ext_vector_type(4))) float f32x4;
typedef __attribute__((ext_vector_type(4))) unsigned int u32x4;
typedef __attribute__((ext_vector_type(2))) unsigned int u32x2;

#define DEV __device__ __forceinline__

constexpr int S_ = 2048, D_ = 640, FH = 864, FP = 896;

DEV float bf2f(u16 v) { return __uint_as_float(((u32)v) << 16); }
DEV float lo2f(u32 v) { return __uint_as_float(v << 16); }
DEV float hi2f(u32 v) { return __uint_as_float(v & 0xffff0000u); }
DEV float rbf(float f) { return (float)(__bf16)f; }
DEV u16 f2bf(float f) { __bf16 b = (__bf16)f; return __builtin_bit_cast(u16, b); }
DEV u32 pack2(float a, float b) { return (u32)f2bf(a) | ((u32)f2bf(b) << 16); }

DEV float wave_sum(float v) {
#pragma unroll
  for (int o = 32; o > 0; o >>= 1) v += __shfl_xor(v, o);
  return v;
}

DEV float gelu_tanh(float x) {
  float y = 0.7978845608028654f * (x + 0.044715f * x * x * x);
  float e = __expf(-2.0f * y);
  return __fdividef(x, 1.0f + e);
}

constexpr int PRE_KV_BLOCKS = 320, PRE_WIN_BLOCKS = 270, PRE_WOUT_BLOCKS = 140, PRE_PAD_BLOCKS = 4;

__global__ __launch_bounds__(256) void pre_kernel(const u16* __restrict__ k, const u16* __restrict__ v,
                                                  const u16* __restrict__ shift_k, const u16* __restrict__ w_in,
                                                  const u16* __restrict__ w_out, u16* __restrict__ kvT,
                                                  u16* __restrict__ w_inT, u16* __restrict__ w_outT) {
  __shared__ u16 tile[64][72];
  const int t = threadIdx.x;
  int b = blockIdx.x;
  if (b < PRE_KV_BLOCKS) {
    const int s0 = (b & 31) * 64, d0 = (b >> 5) * 64;
    const int c8 = t & 7, r0 = t >> 3;
    const int d = d0 + c8 * 8;
    float sk[8][4];
#pragma unroll
    for (int j = 0; j < 4; ++j) {
      u32x4 w = *(const u32x4*)(shift_k + (d + 2 * j) * 4);
      sk[2 * j][0] = lo2f(w[0]); sk[2 * j][1] = hi2f(w[0]); sk[2 * j][2] = lo2f(w[1]); sk[2 * j][3] = hi2f(w[1]);
      sk[2 * j + 1][0] = lo2f(w[2]); sk[2 * j + 1][1] = hi2f(w[2]); sk[2 * j + 1][2] = lo2f(w[3]); sk[2 * j + 1][3] = hi2f(w[3]);
    }
#pragma unroll
    for (int h = 0; h < 2; ++h) {
      const int r = r0 + 32 * h;
      const int s = s0 + r;
      u32x4 kk[4];
#pragma unroll
      for (int j = 0; j < 4; ++j) {
        if (s - j >= 0) kk[j] = *(const u32x4*)(k + (size_t)(s - j) * D_ + d);
        else kk[j] = (u32x4){0u, 0u, 0u, 0u};
      }
      u32x4 vv = *(const u32x4*)(v + (size_t)s * D_ + d);
#pragma unroll
      for (int e = 0; e < 8; ++e) {
        float acc = 0.f;
#pragma unroll
        for (int tt = 0; tt < 4; ++tt) {
          u32 w = kk[3 - tt][e >> 1];
          float kf = (e & 1) ? hi2f(w) : lo2f(w);
          float p = rbf(kf * sk[e][3 - tt]);
          acc = rbf(acc + p);
        }
        u32 w = vv[e >> 1];
        float vf = (e & 1) ? hi2f(w) : lo2f(w);
        tile[c8 * 8 + e][r] = f2bf(acc * vf);
      }
    }
    __syncthreads();
#pragma unroll
    for (int h = 0; h < 2; ++h) {
      int idx = t + 256 * h;
      int dr = idx >> 3, ch = idx & 7;
      u32x4 o = *(const u32x4*)(&tile[dr][ch * 8]);
      *(u32x4*)(kvT + (size_t)(d0 + dr) * S_ + s0 + ch * 8) = o;
    }
    return;
  }
  b -= PRE_KV_BLOCKS;
  if (b < PRE_WIN_BLOCKS + PRE_WOUT_BLOCKS) {
    const u16* src; u16* dst; int ld_src, ld_dst, r0, c0, rmax, drow0;
    if (b < PRE_WIN_BLOCKS) {
      int kt = b % 10, nt = b / 10;
      src = w_in; ld_src = 2 * FH; r0 = kt * 64; c0 = nt * 64; rmax = D_;
      dst = w_inT; ld_dst = D_; drow0 = 0;
    } else {
      int bb = b - PRE_WIN_BLOCKS;
      int kt = bb % 14, dt = bb / 14;
      src = w_out; ld_src = D_; r0 = kt * 64; c0 = dt * 64; rmax = FH;
      dst = w_outT; ld_dst = FP; drow0 = 1;
    }
#pragma unroll
    for (int h = 0; h < 2; ++h) {
      int idx = t + 256 * h;
      int r = idx >> 3, ch = idx & 7;
      u32x4 w = (u32x4){0u, 0u, 0u, 0u};
      if (r0 + r < rmax) w = *(const u32x4*)(src + (size_t)(r0 + r) * ld_src + c0 + ch * 8);
#pragma unroll
      for (int e = 0; e < 4; ++e) {
        tile[ch * 8 + 2 * e][r] = (u16)(w[e] & 0xffffu);
        tile[ch * 8 + 2 * e + 1][r] = (u16)(w[e] >> 16);
      }
    }
    __syncthreads();
#pragma unroll
    for (int h = 0; h < 2; ++h) {
      int idx = t + 256 * h;
      int cr = idx >> 3, ch = idx & 7;
      int n = c0 + cr;
      int drow = drow0 ? n : (n < FH ? n : n + 32);
      u32x4 o = *(const u32x4*)(&tile[cr][ch * 8]);
      *(u32x4*)(dst + (size_t)drow * ld_dst + r0 + ch * 8) = o;
    }
    return;
  }
  b -= PRE_WIN_BLOCKS + PRE_WOUT_BLOCKS;
  {
    for (int i = b * 256 + t; i < 5120; i += PRE_PAD_BLOCKS * 256) {
      int row = i / 80, ch = i % 80;
      int rr = row < 32 ? FH + row : FP + FH + (row - 32);
      *(u32x4*)(w_inT + (size_t)rr * D_ + ch * 8) = (u32x4){0u, 0u, 0u, 0u};
    }
  }
}

constexpr int CV_E_BYTES = 2448 * 16;
constexpr int CV_X_BYTES = 32 * 272;

__global__ __launch_bounds__(256) void conv_kernel(const u16* __restrict__ kvT, const u16* __restrict__ ssm_k,
                                                   const u16* __restrict__ ssm_D, u16* __restrict__ YT) {
  __shared__ __attribute__((aligned(16))) char smem[CV_E_BYTES + CV_X_BYTES];
  char* Et = smem;
  char* Xp = smem + CV_E_BYTES;
  const int t = threadIdx.x, lane = t & 63;
  const int wave = __builtin_amdgcn_readfirstlane(t >> 6);
  const int d = blockIdx.x;
  const u16* h = ssm_k + (size_t)d * S_;
  const u16* x = kvT + (size_t)d * S_;
  {
    u32x4 w1 = *(const u32x4*)(h + 8 * t);
    u32x4 w0 = (u32x4){0u, 0u, 0u, 0u};
    if (t > 0) w0 = *(const u32x4*)(h + 8 * t - 8);
    u32 w[8] = {w0[0], w0[1], w0[2], w0[3], w1[0], w1[1], w1[2], w1[3]};
    char* base = Et + (144 + 9 * t) * 16;
#pragma unroll
    for (int r = 0; r < 8; ++r) {
      u32x4 g;
#pragma unroll
      for (int dd = 0; dd < 4; ++dd) {
        if ((r & 1) == 0) {
          int p = (8 + r - 2 * dd) >> 1;
          g[dd] = (w[p] & 0xffffu) | (w[p - 1] & 0xffff0000u);
        } else {
          int p = (8 + r - 2 * dd - 1) >> 1;
          g[dd] = (w[p] >> 16) | (w[p] << 16);
        }
      }
      *(u32x4*)(base + r * 16) = g;
    }
    if (t < 144) *(u32x4*)(Et + t * 16) = (u32x4){0u, 0u, 0u, 0u};
    u32x4 xv = *(const u32x4*)(x + 8 * t);
    *(u32x4*)(Xp + (16 + (t >> 4)) * 272 + (t & 15) * 16) = xv;
    for (int i = t; i < 16 * 17; i += 256) *(u32x4*)(Xp + i * 16) = (u32x4){0u, 0u, 0u, 0u};
  }
  __syncthreads();
  const int p = wave & 1, hk = wave >> 1;
  const int m = lane & 15, kg = lane >> 4;
  const char* abase = Et + (m + (m >> 3) - 9 * kg) * 16 + 288 * p + 2304;
  const char* bbase = Xp + (m + 16) * 272 + kg * 16;
  f32x4 acc[4];
#pragma unroll
  for (int i = 0; i < 4; ++i) acc[i] = (f32x4){0.f, 0.f, 0.f, 0.f};
  bf16x8 F[4];
  const int D0 = 8 * hk;
  F[1] = *(const bf16x8*)(abase + 576 * (4 * D0 - 3));
  F[2] = *(const bf16x8*)(abase + 576 * (4 * D0 - 2));
  F[3] = *(const bf16x8*)(abase + 576 * (4 * D0 - 1));
  for (int dl = 0; dl < 8; ++dl) {
    const int Dl = D0 + dl;
    const char* ab = abase + 576 * 4 * Dl;
    const char* bb = bbase - 272 * Dl;
#pragma unroll
    for (int qq = 0; qq < 4; ++qq) {
      const int q = 3 - qq;
      F[qq] = *(const bf16x8*)(ab + 576 * qq);
      bf16x8 B = *(const bf16x8*)(bb + 64 * q);
#pragma unroll
      for (int i = 0; i < 4; ++i) {
        acc[i] = __builtin_amdgcn_mfma_f32_16x16x32_bf16(F[(i - q) & 3], B, acc[i], 0, 0, 0);
      }
    }
  }
  __syncthreads();
  float* yp = (float*)Et;
  u16* orow = (u16*)(Et + 8704);
  const int a = m, g = kg;
  if (hk == 1) {
#pragma unroll
    for (int i = 0; i < 4; ++i) {
      int mi = p + 2 * i;
      *(f32x4*)(yp + a * 132 + 16 * mi + 4 * g) = acc[i];
    }
  }
  __syncthreads();
  if (hk == 0) {
    const float Dv = bf2f(ssm_D[d]);
#pragma unroll
    for (int i = 0; i < 4; ++i) {
      int mi = p + 2 * i;
      f32x4 o = *(const f32x4*)(yp + a * 132 + 16 * mi + 4 * g);
      u32x2 xv = *(const u32x2*)(Xp + (a + 16) * 272 + (16 * mi + 4 * g) * 2);
      float xs[4] = {lo2f(xv[0]), hi2f(xv[0]), lo2f(xv[1]), hi2f(xv[1])};
      float res[4];
#pragma unroll
      for (int r = 0; r < 4; ++r) {
        float y = rbf(acc[i][r] + o[r]);
        float kd = rbf(xs[r] * Dv);
        res[r] = y + kd;
      }
      u32x2 ov = {pack2(res[0], res[1]), pack2(res[2], res[3])};
      *(u32x2*)(orow + 128 * a + 16 * mi + 4 * g) = ov;
    }
  }
  __syncthreads();
  *(u32x4*)(YT + (size_t)d * S_ + 8 * t) = *(const u32x4*)(orow + 8 * t);
}

DEV void ln10(const float (&v)[10], float (&o)[10]) {
  float s = 0.f;
#pragma unroll
  for (int i = 0; i < 10; ++i) s += v[i];
  s = wave_sum(s);
  float mean = s * (1.0f / 640.0f);
  float q = 0.f;
#pragma unroll
  for (int i = 0; i < 10; ++i) { float dd = v[i] - mean; q += dd * dd; }
  q = wave_sum(q);
  float var = rbf(q * (1.0f / 640.0f));
  float mb = rbf(mean);
  float den = rbf(sqrtf(rbf(var + 1e-5f)));
  float inv = 1.0f / den;
#pragma unroll
  for (int i = 0; i < 10; ++i) o[i] = rbf(rbf(v[i] - mb) * inv);
}

__global__ __launch_bounds__(512) void rowop_kernel(const u16* __restrict__ xin, const u16* __restrict__ YT,
                                                    u16* __restrict__ xo, u16* __restrict__ xn) {
  __shared__ u16 yt[8][648];
  const int t = threadIdx.x, lane = t & 63, r = t >> 6;
  const int s0 = blockIdx.x * 8;
  for (int d = t; d < D_; d += 512) {
    u32x4 w = *(const u32x4*)(YT + (size_t)d * S_ + s0);
#pragma unroll
    for (int e = 0; e < 4; ++e) {
      yt[2 * e][d] = (u16)(w[e] & 0xffffu);
      yt[2 * e + 1][d] = (u16)(w[e] >> 16);
    }
  }
  float v[10];
  const u16* xr = xin + (size_t)(s0 + r) * D_;
#pragma unroll
  for (int i = 0; i < 5; ++i) {
    u32 w = *(const u32*)(xr + 2 * lane + 128 * i);
    v[2 * i] = lo2f(w); v[2 * i + 1] = hi2f(w);
  }
  float a[10];
  ln10(v, a);
  __syncthreads();
  float x1[10];
#pragma unroll
  for (int i = 0; i < 5; ++i) {
    u32 w = *(const u32*)(&yt[r][2 * lane + 128 * i]);
    x1[2 * i] = rbf(v[2 * i] + rbf(a[2 * i] * lo2f(w)));
    x1[2 * i + 1] = rbf(v[2 * i + 1] + rbf(a[2 * i + 1] * hi2f(w)));
  }
  float n1[10];
  ln10(x1, n1);
  u16* xor_ = xo + (size_t)(s0 + r) * D_;
  u16* xnr = xn + (size_t)(s0 + r) * D_;
#pragma unroll
  for (int i = 0; i < 5; ++i) {
    *(u32*)(xor_ + 2 * lane + 128 * i) = pack2(x1[2 * i], x1[2 * i + 1]);
    *(u32*)(xnr + 2 * lane + 128 * i) = pack2(n1[2 * i], n1[2 * i + 1]);
  }
}

template <int N> DEV void wait_vm() {
  asm volatile("s_waitcnt vmcnt(%0)" ::"n"(N) : "memory");
}

template <int BM, int BN, int NS, int NK, bool SWAP>
DEV void gemm_core(const u16* __restrict__ A, int lda, const u16* __restrict__ Bt, int ldb, char* lds,
                   f32x4 (&acc)[BM / 64][BN / 16]) {
  constexpr int T = (BM + BN) / 8;
  constexpr int NI = (T + 3) / 4;
  constexpr int REM = T % 4;
  constexpr int STAGE = (BM + BN) * 128;
  constexpr int MI = BM / 64, NJ = BN / 16;
  const int t = threadIdx.x, lane = t & 63;
  const int wave = __builtin_amdgcn_readfirstlane(t >> 6);
  const u16* src[NI];
#pragma unroll
  for (int j = 0; j < NI; ++j) {
    int i = wave + 4 * j;
    if (i >= T) i = T - 1;
    int row = 8 * i + (lane >> 3);
    int cg = (lane & 7) ^ (row & 7);
    src[j] = (8 * i < BM) ? (A + (size_t)row * lda + cg * 8) : (Bt + (size_t)(row - BM) * ldb + cg * 8);
  }
  const bool full = (REM == 0) || (wave < REM);
  auto issue = [&](int kt) {
    char* dst = lds + (kt % NS) * STAGE + wave * 1024;
#pragma unroll
    for (int j = 0; j < NI; ++j) {
      if (j < NI - 1 || full)
        __builtin_amdgcn_global_load_lds((const u32*)(src[j] + kt * 64), (u32*)(dst + j * 4096), 16, 0, 0);
    }
  };
#pragma unroll
  for (int s = 0; s < NS - 1; ++s) issue(s);
  const int fr = lane & 15, fq = lane >> 4;
  int aoff[MI], boff[NJ];
#pragma unroll
  for (int mi = 0; mi < MI; ++mi) aoff[mi] = (wave * (BM / 4) + 16 * mi + fr) * 128;
#pragma unroll
  for (int nj = 0; nj < NJ; ++nj) boff[nj] = (BM + 16 * nj + fr) * 128;
  const int sw = fr & 7;
  auto compute = [&](int kt) {
    const char* base = lds + (kt % NS) * STAGE;
#pragma unroll
    for (int kk = 0; kk < 2; ++kk) {
      const int ch = ((4 * kk + fq) ^ sw) * 16;
      bf16x8 af[MI], bfr[NJ];
#pragma unroll
      for (int mi = 0; mi < MI; ++mi) af[mi] = *(const bf16x8*)(base + aoff[mi] + ch);
#pragma unroll
      for (int nj = 0; nj < NJ; ++nj) bfr[nj] = *(const bf16x8*)(base + boff[nj] + ch);
#pragma unroll
      for (int mi = 0; mi < MI; ++mi)
#pragma unroll
        for (int nj = 0; nj < NJ; ++nj) {
          if (SWAP) acc[mi][nj] = __builtin_amdgcn_mfma_f32_16x16x32_bf16(bfr[nj], af[mi], acc[mi][nj], 0, 0, 0);
          else acc[mi][nj] = __builtin_amdgcn_mfma_f32_16x16x32_bf16(af[mi], bfr[nj], acc[mi][nj], 0, 0, 0);
        }
    }
  };
  constexpr int MAINK = NK - (NS - 2) > 0 ? NK - (NS - 2) : 0;
  for (int kt = 0; kt < MAINK; ++kt) {
    if (full) wait_vm<NI*(NS - 2)>(); else wait_vm<(NI - 1) * (NS - 2)>();
    __builtin_amdgcn_s_barrier();
    if (kt + NS - 1 < NK) issue(kt + NS - 1);
    compute(kt);
  }
#pragma unroll
  for (int r = 0; r < NS - 2; ++r) {
    const int kt = MAINK + r;
    if (kt < NK) {
      constexpr int dummy = 0; (void)dummy;
      const int rem = NK - 1 - kt;
      if (rem == 0) wait_vm<0>();
      else if (rem == 1) { if (full) wait_vm<NI>(); else wait_vm<NI - 1>(); }
      else if (rem == 2) { if (full) wait_vm<2 * NI>(); else wait_vm<2 * (NI - 1)>(); }
      else if (rem == 3) { if (full) wait_vm<3 * NI>(); else wait_vm<3 * (NI - 1)>(); }
      else wait_vm<0>();
      __builtin_amdgcn_s_barrier();
      compute(kt);
    }
  }
}

constexpr int G1_NS = 4;
__global__ __launch_bounds__(256) void gemm1_kernel(const u16* __restrict__ xn, const u16* __restrict__ w_inT,
                                                    u16* __restrict__ z1, u16* __restrict__ z2T,
                                                    float2* __restrict__ part) {
  __shared__ __attribute__((aligned(16))) char lds[G1_NS * 240 * 128];
  const int b = blockIdx.x, xcd = b & 7, idx = b >> 3;
  const int mt = (xcd >> 1) * 4 + (idx & 3);
  const int nt = (xcd & 1) * 8 + (idx >> 2);
  const int m0 = mt * 128, n0 = nt * 112;
  const int lane = threadIdx.x & 63, wave = threadIdx.x >> 6;
  const int fr = lane & 15, fq = lane >> 4;
  f32x4 acc[2][7];
#pragma unroll
  for (int i = 0; i < 2; ++i)
#pragma unroll
    for (int j = 0; j < 7; ++j) acc[i][j] = (f32x4){0.f, 0.f, 0.f, 0.f};
  if (nt < 8) {
    gemm_core<128, 112, G1_NS, 10, true>(xn + (size_t)m0 * D_, D_, w_inT + (size_t)n0 * D_, D_, lds, acc);
#pragma unroll
    for (int mi = 0; mi < 2; ++mi) {
      const int s = m0 + wave * 32 + 16 * mi + fr;
#pragma unroll
      for (int nj = 0; nj < 7; ++nj) {
        const int f = n0 + 16 * nj + 4 * fq;
        float z[4];
#pragma unroll
        for (int r = 0; r < 4; ++r) z[r] = gelu_tanh(rbf(acc[mi][nj][r]));
        u32x2 o = {pack2(z[0], z[1]), pack2(z[2], z[3])};
        *(u32x2*)(z1 + (size_t)s * FP + f) = o;
      }
    }
  } else {
    gemm_core<128, 112, G1_NS, 10, false>(xn + (size_t)m0 * D_, D_, w_inT + (size_t)n0 * D_, D_, lds, acc);
    const int nt2 = nt - 8;
    const int f0 = nt2 * 112;
#pragma unroll
    for (int mi = 0; mi < 2; ++mi) {
      const int s = m0 + wave * 32 + 16 * mi + 4 * fq;
      float sum[4] = {0.f, 0.f, 0.f, 0.f}, sq[4] = {0.f, 0.f, 0.f, 0.f};
#pragma unroll
      for (int nj = 0; nj < 7; ++nj) {
        const int f = f0 + 16 * nj + fr;
        float z[4];
#pragma unroll
        for (int r = 0; r < 4; ++r) z[r] = rbf(gelu_tanh(rbf(acc[mi][nj][r])));
        u32x2 o = {pack2(z[0], z[1]), pack2(z[2], z[3])};
        *(u32x2*)(z2T + (size_t)f * S_ + s) = o;
        if (f < FH) {
#pragma unroll
          for (int r = 0; r < 4; ++r) { sum[r] += z[r]; sq[r] += z[r] * z[r]; }
        }
      }
#pragma unroll
      for (int r = 0; r < 4; ++r) {
#pragma unroll
        for (int o = 1; o < 16; o <<= 1) {
          sum[r] += __shfl_xor(sum[r], o);
          sq[r] += __shfl_xor(sq[r], o);
        }
      }
      if (fr == 0) {
#pragma unroll
        for (int r = 0; r < 4; ++r) part[(size_t)nt2 * S_ + s + r] = make_float2(sum[r], sq[r]);
      }
    }
  }
}

__global__ __launch_bounds__(256) void norm_kernel(const u16* __restrict__ z2T, const float2* __restrict__ part,
                                                   u16* __restrict__ z2nT) {
  __shared__ float sm[256], si[256];
  const int t = threadIdx.x;
  const int s0 = (blockIdx.x & 7) * 256, f0 = (blockIdx.x >> 3) * 32;
  {
    float sum = 0.f, sq = 0.f;
#pragma unroll
    for (int j = 0; j < 8; ++j) { float2 p = part[(size_t)j * S_ + s0 + t]; sum += p.x; sq += p.y; }
    float mean = sum * (1.0f / FH);
    float var = fmaxf(sq * (1.0f / FH) - mean * mean, 0.f);
    sm[t] = rbf(mean);
    si[t] = 1.0f / rbf(sqrtf(rbf(rbf(var) + 1e-5f)));
  }
  __syncthreads();
  const int sc = t & 31, fr = t >> 5;
  float mm[8], ii[8];
#pragma unroll
  for (int e = 0; e < 8; ++e) { mm[e] = sm[sc * 8 + e]; ii[e] = si[sc * 8 + e]; }
#pragma unroll
  for (int h = 0; h < 4; ++h) {
    const int f = f0 + fr + 8 * h;
    u32x4 w = *(const u32x4*)(z2T + (size_t)f * S_ + s0 + sc * 8);
    u32x4 o;
#pragma unroll
    for (int e = 0; e < 4; ++e)
      o[e] = pack2(rbf(lo2f(w[e]) - mm[2 * e]) * ii[2 * e], rbf(hi2f(w[e]) - mm[2 * e + 1]) * ii[2 * e + 1]);
    *(u32x4*)(z2nT + (size_t)f * S_ + s0 + sc * 8) = o;
  }
}

constexpr int G2_NS = 5;
__global__ __launch_bounds__(256) void gemm2_kernel(const u16* __restrict__ w_sp, const u16* __restrict__ z2nT,
                                                    const u16* __restrict__ b_sp, const u16* __restrict__ z1,
                                                    u16* __restrict__ u) {
  __shared__ __attribute__((aligned(16))) char lds[G2_NS * 176 * 128];
  const int b = blockIdx.x, xcd = b & 7, idx = b >> 3;
  const int mt = (xcd >> 1) * 8 + (idx & 7);
  const int nt = (xcd & 1) * 4 + (idx >> 3);
  const int m0 = mt * 64, n0 = nt * 112;
  const int lane = threadIdx.x & 63, wave = threadIdx.x >> 6;
  const int fr = lane & 15, fq = lane >> 4;
  f32x4 acc[1][7];
#pragma unroll
  for (int j = 0; j < 7; ++j) acc[0][j] = (f32x4){0.f, 0.f, 0.f, 0.f};
  gemm_core<64, 112, G2_NS, 32, true>(w_sp + (size_t)m0 * S_, S_, z2nT + (size_t)n0 * S_, S_, lds, acc);
  const int tr = m0 + wave * 16 + fr;
  const float bias = bf2f(b_sp[tr]);
#pragma unroll
  for (int nj = 0; nj < 7; ++nj) {
    const int f = n0 + 16 * nj + 4 * fq;
    u32x2 o = {0u, 0u};
    if (f < FH) {
      u32x2 zz = *(const u32x2*)(z1 + (size_t)tr * FP + f);
      float g0 = rbf(rbf(acc[0][nj][0]) + bias), g1 = rbf(rbf(acc[0][nj][1]) + bias);
      float g2 = rbf(rbf(acc[0][nj][2]) + bias), g3 = rbf(rbf(acc[0][nj][3]) + bias);
      o[0] = pack2(lo2f(zz[0]) * g0, hi2f(zz[0]) * g1);
      o[1] = pack2(lo2f(zz[1]) * g2, hi2f(zz[1]) * g3);
    }
    *(u32x2*)(u + (size_t)tr * FP + f) = o;
  }
}

constexpr int G3_NS = 6;
__global__ __launch_bounds__(256) void gemm3_kernel(const u16* __restrict__ u, const u16* __restrict__ w_outT,
                                                    const u16* __restrict__ xres, u16* __restrict__ out) {
  __shared__ __attribute__((aligned(16))) char lds[G3_NS * 144 * 128];
  const int b = blockIdx.x, xcd = b & 7, idx = b >> 3;
  const int mt = (xcd >> 1) * 8 + (idx & 7);
  const int nt = (xcd & 1) * 4 + (idx >> 3);
  const int m0 = mt * 64, n0 = nt * 80;
  const int lane = threadIdx.x & 63, wave = threadIdx.x >> 6;
  const int fr = lane & 15, fq = lane >> 4;
  f32x4 acc[1][5];
#pragma unroll
  for (int j = 0; j < 5; ++j) acc[0][j] = (f32x4){0.f, 0.f, 0.f, 0.f};
  gemm_core<64, 80, G3_NS, 14, true>(u + (size_t)m0 * FP, FP, w_outT + (size_t)n0 * FP, FP, lds, acc);
  const int s = m0 + wave * 16 + fr;
#pragma unroll
  for (int nj = 0; nj < 5; ++nj) {
    const int dcol = n0 + 16 * nj + 4 * fq;
    u32x2 xx = *(const u32x2*)(xres + (size_t)s * D_ + dcol);
    u32x2 o;
    o[0] = pack2(lo2f(xx[0]) + rbf(acc[0][nj][0]), hi2f(xx[0]) + rbf(acc[0][nj][1]));
    o[1] = pack2(lo2f(xx[1]) + rbf(acc[0][nj][2]), hi2f(xx[1]) + rbf(acc[0][nj][3]));
    *(u32x2*)(out + (size_t)s * D_ + dcol) = o;
  }
}

extern "C" void kernel_launch(void* const* d_in, const int* in_sizes, int n_in, void* d_out, int out_size,
                              void* d_ws, size_t ws_size, hipStream_t stream) {
  const u16* A_q = (const u16*)d_in[0];
  const u16* A_k = (const u16*)d_in[1];
  const u16* A_v = (const u16*)d_in[2];
  const u16* A_shift_k = (const u16*)d_in[3];
  const u16* A_ssm_k = (const u16*)d_in[4];
  const u16* A_ssm_D = (const u16*)d_in[5];
  const u16* B_w_in = (const u16*)d_in[7];
  const u16* B_w_sp = (const u16*)d_in[8];
  const u16* B_b_sp = (const u16*)d_in[9];
  const u16* B_w_out = (const u16*)d_in[10];
  u16* out = (u16*)d_out;

  char* ws = (char*)d_ws;
  size_t off = 0;
  auto take = [&](size_t bytes) { char* p = ws + off; off += (bytes + 255) & ~(size_t)255; return p; };
  u16* kvT = (u16*)take((size_t)D_ * S_ * 2);
  u16* YT = (u16*)take((size_t)D_ * S_ * 2);
  u16* w_inT = (u16*)take((size_t)2 * FP * D_ * 2);
  u16* w_outT = (u16*)take((size_t)D_ * FP * 2);
  u16* xa = (u16*)take((size_t)S_ * D_ * 2);
  u16* xb = (u16*)take((size_t)S_ * D_ * 2);
  u16* xn = (u16*)take((size_t)S_ * D_ * 2);
  u16* z1 = (u16*)take((size_t)S_ * FP * 2);
  u16* z2T = (u16*)take((size_t)FP * S_ * 2);
  u16* z2nT = (u16*)take((size_t)FP * S_ * 2);
  u16* u = (u16*)take((size_t)S_ * FP * 2);
  float2* part = (float2*)take((size_t)8 * S_ * 8);

  pre_kernel<<<PRE_KV_BLOCKS + PRE_WIN_BLOCKS + PRE_WOUT_BLOCKS + PRE_PAD_BLOCKS, 256, 0, stream>>>(
      A_k, A_v, A_shift_k, B_w_in, B_w_out, kvT, w_inT, w_outT);
  conv_kernel<<<640, 256, 0, stream>>>(kvT, A_ssm_k, A_ssm_D, YT);
  rowop_kernel<<<256, 512, 0, stream>>>(A_q, YT, xa, xn);
  gemm1_kernel<<<256, 256, 0, stream>>>(xn, w_inT, z1, z2T, part);
  norm_kernel<<<224, 256, 0, stream>>>(z2T, part, z2nT);
  gemm2_kernel<<<256, 256, 0, stream>>>(B_w_sp, z2nT, B_b_sp, z1, u);
  gemm3_kernel<<<256, 256, 0, stream>>>(u, w_outT, xa, xb);
  rowop_kernel<<<256, 512, 0, stream>>>(xb, YT, xa, xn);
  gemm1_kernel<<<256, 256, 0, stream>>>(xn, w_inT, z1, z2T, part);
  norm_kernel<<<224, 256, 0, stream>>>(z2T, part, z2nT);
  gemm2_kernel<<<256, 256, 0, stream>>>(B_w_sp, z2nT, B_b_sp, z1, u);
  gemm3_kernel<<<256, 256, 0, stream>>>(u, w_outT, xa, out);
}
```

```cpp
#include <hip/hip_runtime.h>
#include <hip/hip_bf16.h>
#include <stdint.h>

typedef unsigned short u16;
typedef unsigned int u32;
typedef __attribute__((ext_vector_type(8))) short bf16x8;
typedef __attribute__((ext_vector_type(4))) float f32x4;
typedef __attribute__((ext_vector_type(4))) unsigned int u32x4;
typedef __attribute__((ext_vector_type(2))) unsigned int u32x2;

#define DEV __device__ __forceinline__

constexpr int S_ = 2048, D_ = 640, FH = 864, FP = 896;

DEV float bf2f(u16 v) { return __uint_as_float(((u32)v) << 16); }
DEV float lo2f(u32 v) { return __uint_as_float(v << 16); }
DEV float hi2f(u32 v) { return __uint_as_float(v & 0xffff0000u); }
DEV float rbf(float f) { return (float)(__bf16)f; }
DEV u16 f2bf(float f) { __bf16 b = (__bf16)f; return __builtin_bit_cast(u16, b); }
DEV u32 pack2(float a, float b) { return (u32)f2bf(a) | ((u32)f2bf(b) << 16); }

DEV float wave_sum(float v) {
#pragma unroll
  for (int o = 32; o > 0; o >>= 1) v += __shfl_xor(v, o);
  return v;
}

DEV float gelu_tanh(float x) {
  float y = 0.7978845608028654f * (x + 0.044715f * x * x * x);
  float e = __expf(-2.0f * y);
  return __fdividef(x, 1.0f + e);
}

constexpr int PRE_KV_BLOCKS = 320, PRE_WIN_BLOCKS = 270, PRE_WOUT_BLOCKS = 140, PRE_PAD_BLOCKS = 4;

__global__ __launch_bounds__(256) void pre_kernel(const u16* __restrict__ k, const u16* __restrict__ v,
                                                  const u16* __restrict__ shift_k, const u16* __restrict__ w_in,
                                                  const u16* __restrict__ w_out, u16* __restrict__ kvT,
                                                  u16* __restrict__ w_inT, u16* __restrict__ w_outT) {
  __shared__ u16 tile[64][72];
  const int t = threadIdx.x;
  int b = blockIdx.x;
  if (b < PRE_KV_BLOCKS) {
    const int s0 = (b & 31) * 64, d0 = (b >> 5) * 64;
    const int c8 = t & 7, r0 = t >> 3;
    const int d = d0 + c8 * 8;
    float sk[8][4];
#pragma unroll
    for (int j = 0; j < 4; ++j) {
      u32x4 w = *(const u32x4*)(shift_k + (d + 2 * j) * 4);
      sk[2 * j][0] = lo2f(w[0]); sk[2 * j][1] = hi2f(w[0]); sk[2 * j][2] = lo2f(w[1]); sk[2 * j][3] = hi2f(w[1]);
      sk[2 * j + 1][0] = lo2f(w[2]); sk[2 * j + 1][1] = hi2f(w[2]); sk[2 * j + 1][2] = lo2f(w[3]); sk[2 * j + 1][3] = hi2f(w[3]);
    }
#pragma unroll
    for (int h = 0; h < 2; ++h) {
      const int r = r0 + 32 * h;
      const int s = s0 + r;
      u32x4 kk[4];
#pragma unroll
      for (int j = 0; j < 4; ++j) {
        if (s - j >= 0) kk[j] = *(const u32x4*)(k + (size_t)(s - j) * D_ + d);
        else kk[j] = (u32x4){0u, 0u, 0u, 0u};
      }
      u32x4 vv = *(const u32x4*)(v + (size_t)s * D_ + d);
#pragma unroll
      for (int e = 0; e < 8; ++e) {
        float acc = 0.f;
#pragma unroll
        for (int tt = 0; tt < 4; ++tt) {
          u32 w = kk[3 - tt][e >> 1];
          float kf = (e & 1) ? hi2f(w) : lo2f(w);
          float p = rbf(kf * sk[e][3 - tt]);
          acc = rbf(acc + p);
        }
        u32 w = vv[e >> 1];
        float vf = (e & 1) ? hi2f(w) : lo2f(w);
        tile[c8 * 8 + e][r] = f2bf(acc * vf);
      }
    }
    __syncthreads();
#pragma unroll
    for (int h = 0; h < 2; ++h) {
      int idx = t + 256 * h;
      int dr = idx >> 3, ch = idx & 7;
      u32x4 o = *(const u32x4*)(&tile[dr][ch * 8]);
      *(u32x4*)(kvT + (size_t)(d0 + dr) * S_ + s0 + ch * 8) = o;
    }
    return;
  }
  b -= PRE_KV_BLOCKS;
  if (b < PRE_WIN_BLOCKS + PRE_WOUT_BLOCKS) {
    const u16* src; u16* dst; int ld_src, ld_dst, r0, c0, rmax, drow0;
    if (b < PRE_WIN_BLOCKS) {
      int kt = b % 10, nt = b / 10;
      src = w_in; ld_src = 2 * FH; r0 = kt * 64; c0 = nt * 64; rmax = D_;
      dst = w_inT; ld_dst = D_; drow0 = 0;
    } else {
      int bb = b - PRE_WIN_BLOCKS;
      int kt = bb % 14, dt = bb / 14;
      src = w_out; ld_src = D_; r0 = kt * 64; c0 = dt * 64; rmax = FH;
      dst = w_outT; ld_dst = FP; drow0 = 1;
    }
#pragma unroll
    for (int h = 0; h < 2; ++h) {
      int idx = t + 256 * h;
      int r = idx >> 3, ch = idx & 7;
      u32x4 w = (u32x4){0u, 0u, 0u, 0u};
      if (r0 + r < rmax) w = *(const u32x4*)(src + (size_t)(r0 + r) * ld_src + c0 + ch * 8);
#pragma unroll
      for (int e = 0; e < 4; ++e) {
        tile[ch * 8 + 2 * e][r] = (u16)(w[e] & 0xffffu);
        tile[ch * 8 + 2 * e + 1][r] = (u16)(w[e] >> 16);
      }
    }
    __syncthreads();
#pragma unroll
    for (int h = 0; h < 2; ++h) {
      int idx = t + 256 * h;
      int cr = idx >> 3, ch = idx & 7;
      int n = c0 + cr;
      int drow = drow0 ? n : (n < FH ? n : n + 32);
      u32x4 o = *(const u32x4*)(&tile[cr][ch * 8]);
      *(u32x4*)(dst + (size_t)drow * ld_dst + r0 + ch * 8) = o;
    }
    return;
  }
  b -= PRE_WIN_BLOCKS + PRE_WOUT_BLOCKS;
  {
    for (int i = b * 256 + t; i < 5120; i += PRE_PAD_BLOCKS * 256) {
      int row = i / 80, ch = i % 80;
      int rr = row < 32 ? FH + row : FP + FH + (row - 32);
      *(u32x4*)(w_inT + (size_t)rr * D_ + ch * 8) = (u32x4){0u, 0u, 0u, 0u};
    }
  }
}

constexpr int CV_E_BYTES = 2448 * 16;
constexpr int CV_X_BYTES = 32 * 272;

__global__ __launch_bounds__(256) void conv_kernel(const u16* __restrict__ kvT, const u16* __restrict__ ssm_k,
                                                   const u16* __restrict__ ssm_D, u16* __restrict__ YT) {
  __shared__ __attribute__((aligned(16))) char smem[CV_E_BYTES + CV_X_BYTES];
  char* Et = smem;
  char* Xp = smem + CV_E_BYTES;
  const int t = threadIdx.x, lane = t & 63;
  const int wave = __builtin_amdgcn_readfirstlane(t >> 6);
  const int d = blockIdx.x;
  const u16* h = ssm_k + (size_t)d * S_;
  const u16* x = kvT + (size_t)d * S_;
  {
    u32x4 w1 = *(const u32x4*)(h + 8 * t);
    u32x4 w0 = (u32x4){0u, 0u, 0u, 0u};
    if (t > 0) w0 = *(const u32x4*)(h + 8 * t - 8);
    u32 w[8] = {w0[0], w0[1], w0[2], w0[3], w1[0], w1[1], w1[2], w1[3]};
    char* base = Et + (144 + 9 * t) * 16;
#pragma unroll
    for (int r = 0; r < 8; ++r) {
      u32x4 g;
#pragma unroll
      for (int dd = 0; dd < 4; ++dd) {
        if ((r & 1) == 0) {
          int p = (8 + r - 2 * dd) >> 1;
          g[dd] = (w[p] & 0xffffu) | (w[p - 1] & 0xffff0000u);
        } else {
          int p = (8 + r - 2 * dd - 1) >> 1;
          g[dd] = (w[p] >> 16) | (w[p] << 16);
        }
      }
      *(u32x4*)(base + r * 16) = g;
    }
    if (t < 144) *(u32x4*)(Et + t * 16) = (u32x4){0u, 0u, 0u, 0u};
    u32x4 xv = *(const u32x4*)(x + 8 * t);
    *(u32x4*)(Xp + (16 + (t >> 4)) * 272 + (t & 15) * 16) = xv;
    for (int i = t; i < 16 * 17; i += 256) *(u32x4*)(Xp + i * 16) = (u32x4){0u, 0u, 0u, 0u};
  }
  __syncthreads();
  const int p = wave & 1, hk = wave >> 1;
  const int m = lane & 15, kg = lane >> 4;
  const char* abase = Et + (m + (m >> 3) - 9 * kg) * 16 + 288 * p + 2304;
  const char* bbase = Xp + (m + 16) * 272 + kg * 16;
  f32x4 acc[4];
#pragma unroll
  for (int i = 0; i < 4; ++i) acc[i] = (f32x4){0.f, 0.f, 0.f, 0.f};
  bf16x8 F[4];
  const int D0 = 8 * hk;
  F[1] = *(const bf16x8*)(abase + 576 * (4 * D0 - 3));
  F[2] = *(const bf16x8*)(abase + 576 * (4 * D0 - 2));
  F[3] = *(const bf16x8*)(abase + 576 * (4 * D0 - 1));
  for (int dl = 0; dl < 8; ++dl) {
    const int Dl = D0 + dl;
    const char* ab = abase + 576 * 4 * Dl;
    const char* bb = bbase - 272 * Dl;
#pragma unroll
    for (int qq = 0; qq < 4; ++qq) {
      const int q = 3 - qq;
      F[qq] = *(const bf16x8*)(ab + 576 * qq);
      bf16x8 B = *(const bf16x8*)(bb + 64 * q);
#pragma unroll
      for (int i = 0; i < 4; ++i) {
        acc[i] = __builtin_amdgcn_mfma_f32_16x16x32_bf16(F[(i - q) & 3], B, acc[i], 0, 0, 0);
      }
    }
  }
  __syncthreads();
  float* yp = (float*)Et;
  u16* orow = (u16*)(Et + 8704);
  const int a = m, g = kg;
  if (hk == 1) {
#pragma unroll
    for (int i = 0; i < 4; ++i) {
      int mi = p + 2 * i;
      *(f32x4*)(yp + a * 132 + 16 * mi + 4 * g) = acc[i];
    }
  }
  __syncthreads();
  if (hk == 0) {
    const float Dv = bf2f(ssm_D[d]);
#pragma unroll
    for (int i = 0; i < 4; ++i) {
      int mi = p + 2 * i;
      f32x4 o = *(const f32x4*)(yp + a * 132 + 16 * mi + 4 * g);
      u32x2 xv = *(const u32x2*)(Xp + (a + 16) * 272 + (16 * mi + 4 * g) * 2);
      float xs[4] = {lo2f(xv[0]), hi2f(xv[0]), lo2f(xv[1]), hi2f(xv[1])};
      float res[4];
#pragma unroll
      for (int r = 0; r < 4; ++r) {
        float y = rbf(acc[i][r] + o[r]);
        float kd = rbf(xs[r] * Dv);
        res[r] = y + kd;
      }
      u32x2 ov = {pack2(res[0], res[1]), pack2(res[2], res[3])};
      *(u32x2*)(orow + 128 * a + 16 * mi + 4 * g) = ov;
    }
  }
  __syncthreads();
  *(u32x4*)(YT + (size_t)d * S_ + 8 * t) = *(const u32x4*)(orow + 8 * t);
}

DEV void ln10(const float (&v)[10], float (&o)[10]) {
  float s = 0.f;
#pragma unroll
  for (int i = 0; i < 10; ++i) s += v[i];
  s = wave_sum(s);
  float mean = s * (1.0f / 640.0f);
  float q = 0.f;
#pragma unroll
  for (int i = 0; i < 10; ++i) { float dd = v[i] - mean; q += dd * dd; }
  q = wave_sum(q);
  float var = rbf(q * (1.0f / 640.0f));
  float mb = rbf(mean);
  float den = rbf(sqrtf(rbf(var + 1e-5f)));
  float inv = 1.0f / den;
#pragma unroll
  for (int i = 0; i < 10; ++i) o[i] = rbf(rbf(v[i] - mb) * inv);
}

__global__ __launch_bounds__(512) void rowop_kernel(const u16* __restrict__ xin, const u16* __restrict__ YT,
                                                    u16* __restrict__ xo, u16* __restrict__ xn) {
  __shared__ u16 yt[8][648];
  const int t = threadIdx.x, lane = t & 63, r = t >> 6;
  const int s0 = blockIdx.x * 8;
  for (int d = t; d < D_; d += 512) {
    u32x4 w = *(const u32x4*)(YT + (size_t)d * S_ + s0);
#pragma unroll
    for (int e = 0; e < 4; ++e) {
      yt[2 * e][d] = (u16)(w[e] & 0xffffu);
      yt[2 * e + 1][d] = (u16)(w[e] >> 16);
    }
  }
  float v[10];
  const u16* xr = xin + (size_t)(s0 + r) * D_;
#pragma unroll
  for (int i = 0; i < 5; ++i) {
    u32 w = *(const u32*)(xr + 2 * lane + 128 * i);
    v[2 * i] = lo2f(w); v[2 * i + 1] = hi2f(w);
  }
  float a[10];
  ln10(v, a);
  __syncthreads();
  float x1[10];
#pragma unroll
  for (int i = 0; i < 5; ++i) {
    u32 w = *(const u32*)(&yt[r][2 * lane + 128 * i]);
    x1[2 * i] = rbf(v[2 * i] + rbf(a[2 * i] * lo2f(w)));
    x1[2 * i + 1] = rbf(v[2 * i + 1] + rbf(a[2 * i + 1] * hi2f(w)));
  }
  float n1[10];
  ln10(x1, n1);
  u16* xor_ = xo + (size_t)(s0 + r) * D_;
  u16* xnr = xn + (size_t)(s0 + r) * D_;
#pragma unroll
  for (int i = 0; i < 5; ++i) {
    *(u32*)(xor_ + 2 * lane + 128 * i) = pack2(x1[2 * i], x1[2 * i + 1]);
    *(u32*)(xnr + 2 * lane + 128 * i) = pack2(n1[2 * i], n1[2 * i + 1]);
  }
}

template <int N> DEV void wait_vm() {
  asm volatile("s_waitcnt vmcnt(%0)" ::"n"(N) : "memory");
}

template <int BM, int BN, int NS, int NK, bool SWAP>
DEV void gemm_core(const u16* __restrict__ A, int lda, const u16* __restrict__ Bt, int ldb, char* lds,
                   f32x4 (&acc)[BM / 64][BN / 16]) {
  constexpr int T = (BM + BN) / 8;
  constexpr int NI = (T + 3) / 4;
  constexpr int REM = T % 4;
  constexpr int STAGE = (BM + BN) * 128;
  constexpr int MI = BM / 64, NJ = BN / 16;
  const int t = threadIdx.x, lane = t & 63;
  const int wave = __builtin_amdgcn_readfirstlane(t >> 6);
  const u16* src[NI];
#pragma unroll
  for (int j = 0; j < NI; ++j) {
    int i = wave + 4 * j;
    if (i >= T) i = T - 1;
    int row = 8 * i + (lane >> 3);
    int cg = (lane & 7) ^ (row & 7);
    src[j] = (8 * i < BM) ? (A + (size_t)row * lda + cg * 8) : (Bt + (size_t)(row - BM) * ldb + cg * 8);
  }
  const bool full = (REM == 0) || (wave < REM);
  auto issue = [&](int kt) {
    char* dst = lds + (kt % NS) * STAGE + wave * 1024;
#pragma unroll
    for (int j = 0; j < NI; ++j) {
      if (j < NI - 1 || full)
        __builtin_amdgcn_global_load_lds((const u32*)(src[j] + kt * 64), (u32*)(dst + j * 4096), 16, 0, 0);
    }
  };
#pragma unroll
  for (int s = 0; s < NS - 1; ++s) issue(s);
  const int fr = lane & 15, fq = lane >> 4;
  int aoff[MI], boff[NJ];
#pragma unroll
  for (int mi = 0; mi < MI; ++mi) aoff[mi] = (wave * (BM / 4) + 16 * mi + fr) * 128;
#pragma unroll
  for (int nj = 0; nj < NJ; ++nj) boff[nj] = (BM + 16 * nj + fr) * 128;
  const int sw = fr & 7;
  bf16x8 fa[2][2][MI], fb[2][2][NJ];
  auto rd = [&](int kt, bf16x8 (&a)[2][MI], bf16x8 (&bb)[2][NJ]) {
    const char* base = lds + (kt % NS) * STAGE;
#pragma unroll
    for (int kk = 0; kk < 2; ++kk) {
      const int ch = ((4 * kk + fq) ^ sw) * 16;
#pragma unroll
      for (int mi = 0; mi < MI; ++mi) a[kk][mi] = *(const bf16x8*)(base + aoff[mi] + ch);
#pragma unroll
      for (int nj = 0; nj < NJ; ++nj) bb[kk][nj] = *(const bf16x8*)(base + boff[nj] + ch);
    }
  };
  auto mma = [&](bf16x8 (&a)[2][MI], bf16x8 (&bb)[2][NJ]) {
#pragma unroll
    for (int kk = 0; kk < 2; ++kk)
#pragma unroll
      for (int mi = 0; mi < MI; ++mi)
#pragma unroll
        for (int nj = 0; nj < NJ; ++nj) {
          if (SWAP) acc[mi][nj] = __builtin_amdgcn_mfma_f32_16x16x32_bf16(bb[kk][nj], a[kk][mi], acc[mi][nj], 0, 0, 0);
          else acc[mi][nj] = __builtin_amdgcn_mfma_f32_16x16x32_bf16(a[kk][mi], bb[kk][nj], acc[mi][nj], 0, 0, 0);
        }
  };
  auto waitfor = [&](int rem) {
    if (rem <= 0) wait_vm<0>();
    else if (rem == 1) { if (full) wait_vm<NI>(); else wait_vm<NI - 1>(); }
    else if (rem == 2) { if (full) wait_vm<2 * NI>(); else wait_vm<2 * (NI - 1)>(); }
    else if (rem == 3) { if (full) wait_vm<3 * NI>(); else wait_vm<3 * (NI - 1)>(); }
    else if (rem == 4) { if (full) wait_vm<4 * NI>(); else wait_vm<4 * (NI - 1)>(); }
    else wait_vm<0>();
  };
  static_assert(NK % 2 == 0 && NS - 2 <= 4, "");
  constexpr int MAINK = ((NK - (NS - 1)) / 2) * 2;
#define STEP(kt, cur, prev, first)                          \
  {                                                          \
    waitfor((NK - 1 - (kt)) < (NS - 2) ? (NK - 1 - (kt)) : (NS - 2)); \
    __builtin_amdgcn_s_barrier();                            \
    if ((kt) + NS - 1 < NK) issue((kt) + NS - 1);            \
    rd((kt), fa[cur], fb[cur]);                              \
    __builtin_amdgcn_sched_barrier(0);                       \
    if (!(first)) mma(fa[prev], fb[prev]);                   \
  }
  STEP(0, 0, 1, true)
  STEP(1, 1, 0, false)
  for (int kt = 2; kt < MAINK; kt += 2) {
    waitfor(NS - 2);
    __builtin_amdgcn_s_barrier();
    issue(kt + NS - 1);
    rd(kt, fa[0], fb[0]);
    __builtin_amdgcn_sched_barrier(0);
    mma(fa[1], fb[1]);
    waitfor(NS - 2);
    __builtin_amdgcn_s_barrier();
    issue(kt + NS);
    rd(kt + 1, fa[1], fb[1]);
    __builtin_amdgcn_sched_barrier(0);
    mma(fa[0], fb[0]);
  }
#pragma unroll
  for (int kt = MAINK; kt < NK; kt += 2) {
    STEP(kt, 0, 1, false)
    STEP(kt + 1, 1, 0, false)
  }
  mma(fa[1], fb[1]);
#undef STEP
}

constexpr int G1_NS = 4;
__global__ __launch_bounds__(256) void gemm1_kernel(const u16* __restrict__ xn, const u16* __restrict__ w_inT,
                                                    u16* __restrict__ z1, u16* __restrict__ z2T,
                                                    float2* __restrict__ part) {
  __shared__ __attribute__((aligned(16))) char lds[G1_NS * 240 * 128];
  const int b = blockIdx.x, xcd = b & 7, idx = b >> 3;
  const int mt = (xcd >> 1) * 4 + (idx & 3);
  const int nt = (xcd & 1) * 8 + (idx >> 2);
  const int m0 = mt * 128, n0 = nt * 112;
  const int lane = threadIdx.x & 63, wave = threadIdx.x >> 6;
  const int fr = lane & 15, fq = lane >> 4;
  f32x4 acc[2][7];
#pragma unroll
  for (int i = 0; i < 2; ++i)
#pragma unroll
    for (int j = 0; j < 7; ++j) acc[i][j] = (f32x4){0.f, 0.f, 0.f, 0.f};
  if (nt < 8) {
    gemm_core<128, 112, G1_NS, 10, true>(xn + (size_t)m0 * D_, D_, w_inT + (size_t)n0 * D_, D_, lds, acc);
#pragma unroll
    for (int mi = 0; mi < 2; ++mi) {
      const int s = m0 + wave * 32 + 16 * mi + fr;
#pragma unroll
      for (int nj = 0; nj < 7; ++nj) {
        const int f = n0 + 16 * nj + 4 * fq;
        float z[4];
#pragma unroll
        for (int r = 0; r < 4; ++r) z[r] = gelu_tanh(rbf(acc[mi][nj][r]));
        u32x2 o = {pack2(z[0], z[1]), pack2(z[2], z[3])};
        *(u32x2*)(z1 + (size_t)s * FP + f) = o;
      }
    }
  } else {
    gemm_core<128, 112, G1_NS, 10, false>(xn + (size_t)m0 * D_, D_, w_inT + (size_t)n0 * D_, D_, lds, acc);
    const int nt2 = nt - 8;
    const int f0 = nt2 * 112;
#pragma unroll
    for (int mi = 0; mi < 2; ++mi) {
      const int s = m0 + wave * 32 + 16 * mi + 4 * fq;
      float sum[4] = {0.f, 0.f, 0.f, 0.f}, sq[4] = {0.f, 0.f, 0.f, 0.f};
#pragma unroll
      for (int nj = 0; nj < 7; ++nj) {
        const int f = f0 + 16 * nj + fr;
        float z[4];
#pragma unroll
        for (int r = 0; r < 4; ++r) z[r] = rbf(gelu_tanh(rbf(acc[mi][nj][r])));
        u32x2 o = {pack2(z[0], z[1]), pack2(z[2], z[3])};
        *(u32x2*)(z2T + (size_t)f * S_ + s) = o;
        if (f < FH) {
#pragma unroll
          for (int r = 0; r < 4; ++r) { sum[r] += z[r]; sq[r] += z[r] * z[r]; }
        }
      }
#pragma unroll
      for (int r = 0; r < 4; ++r) {
#pragma unroll
        for (int o = 1; o < 16; o <<= 1) {
          sum[r] += __shfl_xor(sum[r], o);
          sq[r] += __shfl_xor(sq[r], o);
        }
      }
      if (fr == 0) {
#pragma unroll
        for (int r = 0; r < 4; ++r) part[(size_t)nt2 * S_ + s + r] = make_float2(sum[r], sq[r]);
      }
    }
  }
}

__global__ __launch_bounds__(256) void norm_kernel(const u16* __restrict__ z2T, const float2* __restrict__ part,
                                                   u16* __restrict__ z2nT) {
  __shared__ float sm[256], si[256];
  const int t = threadIdx.x;
  const int s0 = (blockIdx.x & 7) * 256, f0 = (blockIdx.x >> 3) * 32;
  {
    float sum = 0.f, sq = 0.f;
#pragma unroll
    for (int j = 0; j < 8; ++j) { float2 p = part[(size_t)j * S_ + s0 + t]; sum += p.x; sq += p.y; }
    float mean = sum * (1.0f / FH);
    float var = fmaxf(sq * (1.0f / FH) - mean * mean, 0.f);
    sm[t] = rbf(mean);
    si[t] = 1.0f / rbf(sqrtf(rbf(rbf(var) + 1e-5f)));
  }
  __syncthreads();
  const int sc = t & 31, fr = t >> 5;
  float mm[8], ii[8];
#pragma unroll
  for (int e = 0; e < 8; ++e) { mm[e] = sm[sc * 8 + e]; ii[e] = si[sc * 8 + e]; }
#pragma unroll
  for (int h = 0; h < 4; ++h) {
    const int f = f0 + fr + 8 * h;
    u32x4 w = *(const u32x4*)(z2T + (size_t)f * S_ + s0 + sc * 8);
    u32x4 o;
#pragma unroll
    for (int e = 0; e < 4; ++e)
      o[e] = pack2(rbf(lo2f(w[e]) - mm[2 * e]) * ii[2 * e], rbf(hi2f(w[e]) - mm[2 * e + 1]) * ii[2 * e + 1]);
    *(u32x4*)(z2nT + (size_t)f * S_ + s0 + sc * 8) = o;
  }
}

constexpr int G2_NS = 5;
__global__ __launch_bounds__(256) void gemm2_kernel(const u16* __restrict__ w_sp, const u16* __restrict__ z2nT,
                                                    const u16* __restrict__ b_sp, const u16* __restrict__ z1,
                                                    u16* __restrict__ u) {
  __shared__ __attribute__((aligned(16))) char lds[G2_NS * 176 * 128];
  const int b = blockIdx.x, xcd = b & 7, idx = b >> 3;
  const int mt = (xcd >> 1) * 8 + (idx & 7);
  const int nt = (xcd & 1) * 4 + (idx >> 3);
  const int m0 = mt * 64, n0 = nt * 112;
  const int lane = threadIdx.x & 63, wave = threadIdx.x >> 6;
  const int fr = lane & 15, fq = lane >> 4;
  f32x4 acc[1][7];
#pragma unroll
  for (int j = 0; j < 7; ++j) acc[0][j] = (f32x4){0.f, 0.f, 0.f, 0.f};
  gemm_core<64, 112, G2_NS, 32, true>(w_sp + (size_t)m0 * S_, S_, z2nT + (size_t)n0 * S_, S_, lds, acc);
  const int tr = m0 + wave * 16 + fr;
  const float bias = bf2f(b_sp[tr]);
#pragma unroll
  for (int nj = 0; nj < 7; ++nj) {
    const int f = n0 + 16 * nj + 4 * fq;
    u32x2 o = {0u, 0u};
    if (f < FH) {
      u32x2 zz = *(const u32x2*)(z1 + (size_t)tr * FP + f);
      float g0 = rbf(rbf(acc[0][nj][0]) + bias), g1 = rbf(rbf(acc[0][nj][1]) + bias);
      float g2 = rbf(rbf(acc[0][nj][2]) + bias), g3 = rbf(rbf(acc[0][nj][3]) + bias);
      o[0] = pack2(lo2f(zz[0]) * g0, hi2f(zz[0]) * g1);
      o[1] = pack2(lo2f(zz[1]) * g2, hi2f(zz[1]) * g3);
    }
    *(u32x2*)(u + (size_t)tr * FP + f) = o;
  }
}

constexpr int G3_NS = 6;
__global__ __launch_bounds__(256) void gemm3_kernel(const u16* __restrict__ u, const u16* __restrict__ w_outT,
                                                    const u16* __restrict__ xres, u16* __restrict__ out) {
  __shared__ __attribute__((aligned(16))) char lds[G3_NS * 144 * 128];
  const int b = blockIdx.x, xcd = b & 7, idx = b >> 3;
  const int mt = (xcd >> 1) * 8 + (idx & 7);
  const int nt = (xcd & 1) * 4 + (idx >> 3);
  const int m0 = mt * 64, n0 = nt * 80;
  const int lane = threadIdx.x & 63, wave = threadIdx.x >> 6;
  const int fr = lane & 15, fq = lane >> 4;
  f32x4 acc[1][5];
#pragma unroll
  for (int j = 0; j < 5; ++j) acc[0][j] = (f32x4){0.f, 0.f, 0.f, 0.f};
  gemm_core<64, 80, G3_NS, 14, true>(u + (size_t)m0 * FP, FP, w_outT + (size_t)n0 * FP, FP, lds, acc);
  const int s = m0 + wave * 16 + fr;
#pragma unroll
  for (int nj = 0; nj < 5; ++nj) {
    const int dcol = n0 + 16 * nj + 4 * fq;
    u32x2 xx = *(const u32x2*)(xres + (size_t)s * D_ + dcol);
    u32x2 o;
    o[0] = pack2(lo2f(xx[0]) + rbf(acc[0][nj][0]), hi2f(xx[0]) + rbf(acc[0][nj][1]));
    o[1] = pack2(lo2f(xx[1]) + rbf(acc[0][nj][2]), hi2f(xx[1]) + rbf(acc[0][nj][3]));
    *(u32x2*)(out + (size_t)s * D_ + dcol) = o;
  }
}

extern "C" void kernel_launch(void* const* d_in, const int* in_sizes, int n_in, void* d_out, int out_size,
                              void* d_ws, size_t ws_size, hipStream_t stream) {
  const u16* A_q = (const u16*)d_in[0];
  const u16* A_k = (const u16*)d_in[1];
  const u16* A_v = (const u16*)d_in[2];
  const u16* A_shift_k = (const u16*)d_in[3];
  const u16* A_ssm_k = (const u16*)d_in[4];
  const u16* A_ssm_D = (const u16*)d_in[5];
  const u16* B_w_in = (const u16*)d_in[7];
  const u16* B_w_sp = (const u16*)d_in[8];
  const u16* B_b_sp = (const u16*)d_in[9];
  const u16* B_w_out = (const u16*)d_in[10];
  u16* out = (u16*)d_out;

  char* ws = (char*)d_ws;
  size_t off = 0;
  auto take = [&](size_t bytes) { char* p = ws + off; off += (bytes + 255) & ~(size_t)255; return p; };
  u16* kvT = (u16*)take((size_t)D_ * S_ * 2);
  u16* YT = (u16*)take((size_t)D_ * S_ * 2);
  u16* w_inT = (u16*)take((size_t)2 * FP * D_ * 2);
  u16* w_outT = (u16*)take((size_t)D_ * FP * 2);
  u16* xa = (u16*)take((size_t)S_ * D_ * 2);
  u16* xb = (u16*)take((size_t)S_ * D_ * 2);
  u16* xn = (u16*)take((size_t)S_ * D_ * 2);
  u16* z1 = (u16*)take((size_t)S_ * FP * 2);
  u16* z2T = (u16*)take((size_t)FP * S_ * 2);
  u16* z2nT = (u16*)take((size_t)FP * S_ * 2);
  u16* u = (u16*)take((size_t)S_ * FP * 2);
  float2* part = (float2*)take((size_t)8 * S_ * 8);

  pre_kernel<<<PRE_KV_BLOCKS + PRE_WIN_BLOCKS + PRE_WOUT_BLOCKS + PRE_PAD_BLOCKS, 256, 0, stream>>>(
      A_k, A_v, A_shift_k, B_w_in, B_w_out, kvT, w_inT, w_outT);
  conv_kernel<<<640, 256, 0, stream>>>(kvT, A_ssm_k, A_ssm_D, YT);
  rowop_kernel<<<256, 512, 0, stream>>>(A_q, YT, xa, xn);
  gemm1_kernel<<<256, 256, 0, stream>>>(xn, w_inT, z1, z2T, part);
  norm_kernel<<<224, 256, 0, stream>>>(z2T, part, z2nT);
  gemm2_kernel<<<256, 256, 0, stream>>>(B_w_sp, z2nT, B_b_sp, z1, u);
  gemm3_kernel<<<256, 256, 0, stream>>>(u, w_outT, xa, xb);
  rowop_kernel<<<256, 512, 0, stream>>>(xb, YT, xa, xn);
  gemm1_kernel<<<256, 256, 0, stream>>>(xn, w_inT, z1, z2T, part);
  norm_kernel<<<224, 256, 0, stream>>>(z2T, part, z2nT);
  gemm2_kernel<<<256, 256, 0, stream>>>(B_w_sp, z2nT, B_b_sp, z1, u);
  gemm3_kernel<<<256, 256, 0, stream>>>(u, w_outT, xa, out);
}
```

```cpp
#include <hip/hip_runtime.h>
#include <hip/hip_bf16.h>
#include <stdint.h>

typedef unsigned short u16;
typedef unsigned int u32;
typedef __attribute__((ext_vector_type(8))) short bf16x8;
typedef __attribute__((ext_vector_type(4))) float f32x4;
typedef __attribute__((ext_vector_type(4))) unsigned int u32x4;
typedef __attribute__((ext_vector_type(2))) unsigned int u32x2;

#define DEV __device__ __forceinline__

constexpr int S_ = 2048, D_ = 640, FH = 864, FP = 896;

DEV float bf2f(u16 v) { return __uint_as_float(((u32)v) << 16); }
DEV float lo2f(u32 v) { return __uint_as_float(v << 16); }
DEV float hi2f(u32 v) { return __uint_as_float(v & 0xffff0000u); }
DEV float rbf(float f) { return (float)(__bf16)f; }
DEV u16 f2bf(float f) { __bf16 b = (__bf16)f; return __builtin_bit_cast(u16, b); }
DEV u32 pack2(float a, float b) { return (u32)f2bf(a) | ((u32)f2bf(b) << 16); }

DEV float wave_sum(float v) {
#pragma unroll
  for (int o = 32; o > 0; o >>= 1) v += __shfl_xor(v, o);
  return v;
}

DEV float gelu_tanh(float x) {
  float y = 0.7978845608028654f * (x + 0.044715f * x * x * x);
  float e = __expf(-2.0f * y);
  return __fdividef(x, 1.0f + e);
}

constexpr int PRE_KV_BLOCKS = 320, PRE_WIN_BLOCKS = 270, PRE_WOUT_BLOCKS = 140, PRE_PAD_BLOCKS = 4;

__global__ __launch_bounds__(256) void pre_kernel(const u16* __restrict__ k, const u16* __restrict__ v,
                                                  const u16* __restrict__ shift_k, const u16* __restrict__ w_in,
                                                  const u16* __restrict__ w_out, u16* __restrict__ kvT,
                                                  u16* __restrict__ w_inT, u16* __restrict__ w_outT) {
  __shared__ u16 tile[64][72];
  const int t = threadIdx.x;
  int b = blockIdx.x;
  if (b < PRE_KV_BLOCKS) {
    const int s0 = (b & 31) * 64, d0 = (b >> 5) * 64;
    const int c8 = t & 7, r0 = t >> 3;
    const int d = d0 + c8 * 8;
    float sk[8][4];
#pragma unroll
    for (int j = 0; j < 4; ++j) {
      u32x4 w = *(const u32x4*)(shift_k + (d + 2 * j) * 4);
      sk[2 * j][0] = lo2f(w[0]); sk[2 * j][1] = hi2f(w[0]); sk[2 * j][2] = lo2f(w[1]); sk[2 * j][3] = hi2f(w[1]);
      sk[2 * j + 1][0] = lo2f(w[2]); sk[2 * j + 1][1] = hi2f(w[2]); sk[2 * j + 1][2] = lo2f(w[3]); sk[2 * j + 1][3] = hi2f(w[3]);
    }
#pragma unroll
    for (int h = 0; h < 2; ++h) {
      const int r = r0 + 32 * h;
      const int s = s0 + r;
      u32x4 kk[4];
#pragma unroll
      for (int j = 0; j < 4; ++j) {
        if (s - j >= 0) kk[j] = *(const u32x4*)(k + (size_t)(s - j) * D_ + d);
        else kk[j] = (u32x4){0u, 0u, 0u, 0u};
      }
      u32x4 vv = *(const u32x4*)(v + (size_t)s * D_ + d);
#pragma unroll
      for (int e = 0; e < 8; ++e) {
        float acc = 0.f;
#pragma unroll
        for (int tt = 0; tt < 4; ++tt) {
          u32 w = kk[3 - tt][e >> 1];
          float kf = (e & 1) ? hi2f(w) : lo2f(w);
          float p = rbf(kf * sk[e][3 - tt]);
          acc = rbf(acc + p);
        }
        u32 w = vv[e >> 1];
        float vf = (e & 1) ? hi2f(w) : lo2f(w);
        tile[c8 * 8 + e][r] = f2bf(acc * vf);
      }
    }
    __syncthreads();
#pragma unroll
    for (int h = 0; h < 2; ++h) {
      int idx = t + 256 * h;
      int dr = idx >> 3, ch = idx & 7;
      u32x4 o = *(const u32x4*)(&tile[dr][ch * 8]);
      *(u32x4*)(kvT + (size_t)(d0 + dr) * S_ + s0 + ch * 8) = o;
    }
    return;
  }
  b -= PRE_KV_BLOCKS;
  if (b < PRE_WIN_BLOCKS + PRE_WOUT_BLOCKS) {
    const u16* src; u16* dst; int ld_src, ld_dst, r0, c0, rmax, drow0;
    if (b < PRE_WIN_BLOCKS) {
      int kt = b % 10, nt = b / 10;
      src = w_in; ld_src = 2 * FH; r0 = kt * 64; c0 = nt * 64; rmax = D_;
      dst = w_inT; ld_dst = D_; drow0 = 0;
    } else {
      int bb = b - PRE_WIN_BLOCKS;
      int kt = bb % 14, dt = bb / 14;
      src = w_out; ld_src = D_; r0 = kt * 64; c0 = dt * 64; rmax = FH;
      dst = w_outT; ld_dst = FP; drow0 = 1;
    }
#pragma unroll
    for (int h = 0; h < 2; ++h) {
      int idx = t + 256 * h;
      int r = idx >> 3, ch = idx & 7;
      u32x4 w = (u32x4){0u, 0u, 0u, 0u};
      if (r0 + r < rmax) w = *(const u32x4*)(src + (size_t)(r0 + r) * ld_src + c0 + ch * 8);
#pragma unroll
      for (int e = 0; e < 4; ++e) {
        tile[ch * 8 + 2 * e][r] = (u16)(w[e] & 0xffffu);
        tile[ch * 8 + 2 * e + 1][r] = (u16)(w[e] >> 16);
      }
    }
    __syncthreads();
#pragma unroll
    for (int h = 0; h < 2; ++h) {
      int idx = t + 256 * h;
      int cr = idx >> 3, ch = idx & 7;
      int n = c0 + cr;
      int drow = drow0 ? n : (n < FH ? n : n + 32);
      u32x4 o = *(const u32x4*)(&tile[cr][ch * 8]);
      *(u32x4*)(dst + (size_t)drow * ld_dst + r0 + ch * 8) = o;
    }
    return;
  }
  b -= PRE_WIN_BLOCKS + PRE_WOUT_BLOCKS;
  {
    for (int i = b * 256 + t; i < 5120; i += PRE_PAD_BLOCKS * 256) {
      int row = i / 80, ch = i % 80;
      int rr = row < 32 ? FH + row : FP + FH + (row - 32);
      *(u32x4*)(w_inT + (size_t)rr * D_ + ch * 8) = (u32x4){0u, 0u, 0u, 0u};
    }
  }
}

constexpr int CV_E_BYTES = 2448 * 16;
constexpr int CV_X_BYTES = 32 * 272;

__global__ __launch_bounds__(256) void conv_kernel(const u16* __restrict__ kvT, const u16* __restrict__ ssm_k,
                                                   const u16* __restrict__ ssm_D, u16* __restrict__ YT) {
  __shared__ __attribute__((aligned(16))) char smem[CV_E_BYTES + CV_X_BYTES];
  char* Et = smem;
  char* Xp = smem + CV_E_BYTES;
  const int t = threadIdx.x, lane = t & 63;
  const int wave = __builtin_amdgcn_readfirstlane(t >> 6);
  const int d = blockIdx.x;
  const u16* h = ssm_k + (size_t)d * S_;
  const u16* x = kvT + (size_t)d * S_;
  {
    u32x4 w1 = *(const u32x4*)(h + 8 * t);
    u32x4 w0 = (u32x4){0u, 0u, 0u, 0u};
    if (t > 0) w0 = *(const u32x4*)(h + 8 * t - 8);
    u32 w[8] = {w0[0], w0[1], w0[2], w0[3], w1[0], w1[1], w1[2], w1[3]};
    char* base = Et + (144 + 9 * t) * 16;
#pragma unroll
    for (int r = 0; r < 8; ++r) {
      u32x4 g;
#pragma unroll
      for (int dd = 0; dd < 4; ++dd) {
        if ((r & 1) == 0) {
          int p = (8 + r - 2 * dd) >> 1;
          g[dd] = (w[p] & 0xffffu) | (w[p - 1] & 0xffff0000u);
        } else {
          int p = (8 + r - 2 * dd - 1) >> 1;
          g[dd] = (w[p] >> 16) | (w[p] << 16);
        }
      }
      *(u32x4*)(base + r * 16) = g;
    }
    if (t < 144) *(u32x4*)(Et + t * 16) = (u32x4){0u, 0u, 0u, 0u};
    u32x4 xv = *(const u32x4*)(x + 8 * t);
    *(u32x4*)(Xp + (16 + (t >> 4)) * 272 + (t & 15) * 16) = xv;
    for (int i = t; i < 16 * 17; i += 256) *(u32x4*)(Xp + i * 16) = (u32x4){0u, 0u, 0u, 0u};
  }
  __syncthreads();
  const int p = wave & 1, hk = wave >> 1;
  const int m = lane & 15, kg = lane >> 4;
  const char* abase = Et + (m + (m >> 3) - 9 * kg) * 16 + 288 * p + 2304;
  const char* bbase = Xp + (m + 16) * 272 + kg * 16;
  f32x4 acc[4];
#pragma unroll
  for (int i = 0; i < 4; ++i) acc[i] = (f32x4){0.f, 0.f, 0.f, 0.f};
  bf16x8 F[4];
  const int D0 = 8 * hk;
  F[1] = *(const bf16x8*)(abase + 576 * (4 * D0 - 3));
  F[2] = *(const bf16x8*)(abase + 576 * (4 * D0 - 2));
  F[3] = *(const bf16x8*)(abase + 576 * (4 * D0 - 1));
  for (int dl = 0; dl < 8; ++dl) {
    const int Dl = D0 + dl;
    const char* ab = abase + 576 * 4 * Dl;
    const char* bb = bbase - 272 * Dl;
#pragma unroll
    for (int qq = 0; qq < 4; ++qq) {
      const int q = 3 - qq;
      F[qq] = *(const bf16x8*)(ab + 576 * qq);
      bf16x8 B = *(const bf16x8*)(bb + 64 * q);
#pragma unroll
      for (int i = 0; i < 4; ++i) {
        acc[i] = __builtin_amdgcn_mfma_f32_16x16x32_bf16(F[(i - q) & 3], B, acc[i], 0, 0, 0);
      }
    }
  }
  __syncthreads();
  float* yp = (float*)Et;
  u16* orow = (u16*)(Et + 8704);
  const int a = m, g = kg;
  if (hk == 1) {
#pragma unroll
    for (int i = 0; i < 4; ++i) {
      int mi = p + 2 * i;
      *(f32x4*)(yp + a * 132 + 16 * mi + 4 * g) = acc[i];
    }
  }
  __syncthreads();
  if (hk == 0) {
    const float Dv = bf2f(ssm_D[d]);
#pragma unroll
    for (int i = 0; i < 4; ++i) {
      int mi = p + 2 * i;
      f32x4 o = *(const f32x4*)(yp + a * 132 + 16 * mi + 4 * g);
      u32x2 xv = *(const u32x2*)(Xp + (a + 16) * 272 + (16 * mi + 4 * g) * 2);
      float xs[4] = {lo2f(xv[0]), hi2f(xv[0]), lo2f(xv[1]), hi2f(xv[1])};
      float res[4];
#pragma unroll
      for (int r = 0; r < 4; ++r) {
        float y = rbf(acc[i][r] + o[r]);
        float kd = rbf(xs[r] * Dv);
        res[r] = y + kd;
      }
      u32x2 ov = {pack2(res[0], res[1]), pack2(res[2], res[3])};
      *(u32x2*)(orow + 128 * a + 16 * mi + 4 * g) = ov;
    }
  }
  __syncthreads();
  *(u32x4*)(YT + (size_t)d * S_ + 8 * t) = *(const u32x4*)(orow + 8 * t);
}

DEV void ln10(const float (&v)[10], float (&o)[10]) {
  float s = 0.f;
#pragma unroll
  for (int i = 0; i < 10; ++i) s += v[i];
  s = wave_sum(s);
  float mean = s * (1.0f / 640.0f);
  float q = 0.f;
#pragma unroll
  for (int i = 0; i < 10; ++i) { float dd = v[i] - mean; q += dd * dd; }
  q = wave_sum(q);
  float var = rbf(q * (1.0f / 640.0f));
  float mb = rbf(mean);
  float den = rbf(sqrtf(rbf(var + 1e-5f)));
  float inv = 1.0f / den;
#pragma unroll
  for (int i = 0; i < 10; ++i) o[i] = rbf(rbf(v[i] - mb) * inv);
}

__global__ __launch_bounds__(512) void rowop_kernel(const u16* __restrict__ xin, const u16* __restrict__ YT,
                                                    u16* __restrict__ xo, u16* __restrict__ xn) {
  __shared__ u16 yt[8][648];
  const int t = threadIdx.x, lane = t & 63, r = t >> 6;
  const int s0 = ((blockIdx.x & 7) * 32 + (blockIdx.x >> 3)) * 8;
  for (int d = t; d < D_; d += 512) {
    u32x4 w = *(const u32x4*)(YT + (size_t)d * S_ + s0);
#pragma unroll
    for (int e = 0; e < 4; ++e) {
      yt[2 * e][d] = (u16)(w[e] & 0xffffu);
      yt[2 * e + 1][d] = (u16)(w[e] >> 16);
    }
  }
  float v[10];
  const u16* xr = xin + (size_t)(s0 + r) * D_;
#pragma unroll
  for (int i = 0; i < 5; ++i) {
    u32 w = *(const u32*)(xr + 2 * lane + 128 * i);
    v[2 * i] = lo2f(w); v[2 * i + 1] = hi2f(w);
  }
  float a[10];
  ln10(v, a);
  __syncthreads();
  float x1[10];
#pragma unroll
  for (int i = 0; i < 5; ++i) {
    u32 w = *(const u32*)(&yt[r][2 * lane + 128 * i]);
    x1[2 * i] = rbf(v[2 * i] + rbf(a[2 * i] * lo2f(w)));
    x1[2 * i + 1] = rbf(v[2 * i + 1] + rbf(a[2 * i + 1] * hi2f(w)));
  }
  float n1[10];
  ln10(x1, n1);
  u16* xor_ = xo + (size_t)(s0 + r) * D_;
  u16* xnr = xn + (size_t)(s0 + r) * D_;
#pragma unroll
  for (int i = 0; i < 5; ++i) {
    *(u32*)(xor_ + 2 * lane + 128 * i) = pack2(x1[2 * i], x1[2 * i + 1]);
    *(u32*)(xnr + 2 * lane + 128 * i) = pack2(n1[2 * i], n1[2 * i + 1]);
  }
}

template <int N> DEV void wait_vm() {
  asm volatile("s_waitcnt vmcnt(%0)" ::"n"(N) : "memory");
}

template <int BM, int BN, int NS, int NK, bool SWAP, int KID, int EXTRA, class PRE>
DEV void gemm_core(const u16* __restrict__ A, int lda, const u16* __restrict__ Bt, int ldb, char* lds,
                   f32x4 (&acc)[BM / 64][BN / 16], PRE&& pre) {
  constexpr int T = (BM + BN) / 8;
  constexpr int NI = (T + 3) / 4;
  constexpr int REM = T % 4;
  constexpr int STAGE = (BM + BN) * 128;
  constexpr int MI = BM / 64, NJ = BN / 16;
  const int t = threadIdx.x, lane = t & 63;
  const int wave = __builtin_amdgcn_readfirstlane(t >> 6);
  if (wave >= 4) {
    const int lw = wave - 4;
    const u16* src[NI];
#pragma unroll
    for (int j = 0; j < NI; ++j) {
      int i = lw + 4 * j;
      if (i >= T) i = T - 1;
      int row = 8 * i + (lane >> 3);
      int cg = (lane & 7) ^ (row & 7);
      src[j] = (8 * i < BM) ? (A + (size_t)row * lda + cg * 8) : (Bt + (size_t)(row - BM) * ldb + cg * 8);
    }
    const bool full = (REM == 0) || (lw < REM);
    auto issue = [&](int kt) {
      char* dst = lds + (kt % NS) * STAGE + lw * 1024;
#pragma unroll
      for (int j = 0; j < NI; ++j) {
        if (j < NI - 1 || full)
          __builtin_amdgcn_global_load_lds((const u32*)(src[j] + kt * 64), (u32*)(dst + j * 4096), 16, 0, 0);
      }
    };
    auto waitfor = [&](int rem) {
      if (rem <= 0) wait_vm<0>();
      else if (rem == 1) { if (full) wait_vm<NI>(); else wait_vm<NI - 1>(); }
      else if (rem == 2) { if (full) wait_vm<2 * NI>(); else wait_vm<2 * (NI - 1)>(); }
      else if (rem == 3) { if (full) wait_vm<3 * NI>(); else wait_vm<3 * (NI - 1)>(); }
      else if (rem == 4) { if (full) wait_vm<4 * NI>(); else wait_vm<4 * (NI - 1)>(); }
      else if (rem == 5) { if (full) wait_vm<5 * NI>(); else wait_vm<5 * (NI - 1)>(); }
      else wait_vm<0>();
    };
    auto waitforx = [&](int rem) {
      if (rem <= 0) wait_vm<EXTRA>();
      else if (rem == 1) { if (full) wait_vm<EXTRA + NI>(); else wait_vm<EXTRA + NI - 1>(); }
      else if (rem == 2) { if (full) wait_vm<EXTRA + 2 * NI>(); else wait_vm<EXTRA + 2 * (NI - 1)>(); }
      else if (rem == 3) { if (full) wait_vm<EXTRA + 3 * NI>(); else wait_vm<EXTRA + 3 * (NI - 1)>(); }
      else if (rem == 4) { if (full) wait_vm<EXTRA + 4 * NI>(); else wait_vm<EXTRA + 4 * (NI - 1)>(); }
      else if (rem == 5) { if (full) wait_vm<EXTRA + 5 * NI>(); else wait_vm<EXTRA + 5 * (NI - 1)>(); }
      else wait_vm<0>();
    };
    static_assert(NS - 2 <= 5, "");
#pragma unroll
    for (int s = 0; s < NS - 1; ++s) issue(s);
    constexpr int MAINK = NK - (NS - 1);
    for (int kt = 0; kt < MAINK; ++kt) {
      waitfor(NS - 2);
      __builtin_amdgcn_s_barrier();
      issue(kt + NS - 1);
    }
    pre();
#pragma unroll
    for (int kt = MAINK; kt < NK; ++kt) {
      waitforx(NK - 1 - kt);
      __builtin_amdgcn_s_barrier();
    }
    return;
  }
  const int fr = lane & 15, fq = lane >> 4;
  int aoff[MI], boff[NJ];
#pragma unroll
  for (int mi = 0; mi < MI; ++mi) aoff[mi] = (wave * (BM / 4) + 16 * mi + fr) * 128;
#pragma unroll
  for (int nj = 0; nj < NJ; ++nj) boff[nj] = (BM + 16 * nj + fr) * 128;
  const int sw = fr & 7;
  bf16x8 fa[2][2][MI], fb[2][2][NJ];
  auto rd = [&](int kt, bf16x8 (&a)[2][MI], bf16x8 (&bb)[2][NJ]) {
    const char* base = lds + (kt % NS) * STAGE;
#pragma unroll
    for (int kk = 0; kk < 2; ++kk) {
      const int ch = ((4 * kk + fq) ^ sw) * 16;
#pragma unroll
      for (int mi = 0; mi < MI; ++mi) a[kk][mi] = *(const bf16x8*)(base + aoff[mi] + ch);
#pragma unroll
      for (int nj = 0; nj < NJ; ++nj) bb[kk][nj] = *(const bf16x8*)(base + boff[nj] + ch);
    }
  };
  auto mma = [&](bf16x8 (&a)[2][MI], bf16x8 (&bb)[2][NJ]) {
#pragma unroll
    for (int kk = 0; kk < 2; ++kk)
#pragma unroll
      for (int mi = 0; mi < MI; ++mi)
#pragma unroll
        for (int nj = 0; nj < NJ; ++nj) {
          if (SWAP) acc[mi][nj] = __builtin_amdgcn_mfma_f32_16x16x32_bf16(bb[kk][nj], a[kk][mi], acc[mi][nj], 0, 0, 0);
          else acc[mi][nj] = __builtin_amdgcn_mfma_f32_16x16x32_bf16(a[kk][mi], bb[kk][nj], acc[mi][nj], 0, 0, 0);
        }
  };
  static_assert(NK % 2 == 0, "");
  auto ilv = [&]() {
    constexpr int NR = 2 * (MI + NJ), NM = 2 * MI * NJ;
    if constexpr (NM >= NR) {
      constexpr int PER = NM / NR;
#pragma unroll
      for (int i = 0; i < NR; ++i) {
        __builtin_amdgcn_sched_group_barrier(0x100, 1, 0);
        __builtin_amdgcn_sched_group_barrier(0x008, PER, 0);
      }
      __builtin_amdgcn_sched_group_barrier(0x008, NM - PER * NR, 0);
    } else {
#pragma unroll
      for (int i = 0; i < NM; ++i) {
        __builtin_amdgcn_sched_group_barrier(0x100, 1, 0);
        __builtin_amdgcn_sched_group_barrier(0x008, 1, 0);
      }
      __builtin_amdgcn_sched_group_barrier(0x100, NR - NM, 0);
    }
  };
  auto lgk0 = [&]() { __builtin_amdgcn_sched_barrier(0); asm volatile("s_waitcnt lgkmcnt(0)" ::: "memory"); };
  __builtin_amdgcn_s_barrier();
  rd(0, fa[0], fb[0]);
  __builtin_amdgcn_sched_barrier(0);
  lgk0();
  __builtin_amdgcn_s_barrier();
  rd(1, fa[1], fb[1]);
  __builtin_amdgcn_sched_barrier(0);
  mma(fa[0], fb[0]);
  for (int kt = 2; kt < NK; kt += 2) {
    lgk0();
    __builtin_amdgcn_s_barrier();
    rd(kt, fa[0], fb[0]);
    mma(fa[1], fb[1]);
    ilv();
    lgk0();
    __builtin_amdgcn_s_barrier();
    rd(kt + 1, fa[1], fb[1]);
    mma(fa[0], fb[0]);
    ilv();
  }
  mma(fa[1], fb[1]);
}

DEV float gelu_fast(float x) {
  const float c1 = -2.0f * 0.7978845608028654f * 1.4426950408889634f;
  const float c2 = c1 * 0.044715f;
  float e = __builtin_amdgcn_exp2f(x * __builtin_fmaf(x * x, c2, c1));
  return x * __builtin_amdgcn_rcpf(1.0f + e);
}
DEV void lds_barrier() { asm volatile("s_waitcnt lgkmcnt(0)" ::: "memory"); __builtin_amdgcn_s_barrier(); asm volatile("" ::: "memory"); }

constexpr int G1_NS = 4;
__global__ __launch_bounds__(512) void gemm1_kernel(const u16* __restrict__ xn, const u16* __restrict__ w_inT,
                                                    u16* __restrict__ z1, u16* __restrict__ z2T,
                                                    float2* __restrict__ part) {
  __shared__ __attribute__((aligned(16))) char lds[G1_NS * 240 * 128];
  const int b = blockIdx.x, xcd = b & 7, idx = b >> 3;
  const int mt = (xcd >> 1) * 4 + (idx & 3);
  const int nt = (xcd & 1) * 8 + (idx >> 2);
  const int m0 = mt * 128, n0 = nt * 112;
  const int t = threadIdx.x, lane = t & 63, wave = t >> 6;
  const int fr = lane & 15, fq = lane >> 4;
  f32x4 acc[2][7];
#pragma unroll
  for (int i = 0; i < 2; ++i)
#pragma unroll
    for (int j = 0; j < 7; ++j) acc[i][j] = (f32x4){0.f, 0.f, 0.f, 0.f};
  char* st = lds + (10 % G1_NS) * (240 * 128);
  if (nt < 8) {
    gemm_core<128, 112, G1_NS, 10, true, 1, 0>(xn + (size_t)m0 * D_, D_, w_inT + (size_t)n0 * D_, D_, lds, acc, [] {});
    if (wave < 4) {
#pragma unroll
      for (int mi = 0; mi < 2; ++mi)
#pragma unroll
        for (int nj = 0; nj < 7; ++nj) {
          u32x2 o = {pack2(acc[mi][nj][0], acc[mi][nj][1]), pack2(acc[mi][nj][2], acc[mi][nj][3])};
          *(u32x2*)(st + (wave * 32 + 16 * mi + fr) * 240 + nj * 32 + fq * 8) = o;
        }
    }
    lds_barrier();
#pragma unroll
    for (int i = 0; i < 4; ++i) {
      const int c = t + 512 * i;
      if (c < 1792) {
        const int row = c / 14, ch = c - row * 14;
        u32x4 w = *(const u32x4*)(st + row * 240 + ch * 16);
        u32x4 o;
#pragma unroll
        for (int e = 0; e < 4; ++e) o[e] = pack2(gelu_fast(lo2f(w[e])), gelu_fast(hi2f(w[e])));
        *(u32x4*)(z1 + (size_t)(m0 + row) * FP + n0 + ch * 8) = o;
      }
    }
  } else {
    gemm_core<128, 112, G1_NS, 10, false, 1, 0>(xn + (size_t)m0 * D_, D_, w_inT + (size_t)n0 * D_, D_, lds, acc, [] {});
    const int nt2 = nt - 8;
    const int f0 = nt2 * 112;
    if (wave < 4) {
#pragma unroll
      for (int mi = 0; mi < 2; ++mi)
#pragma unroll
        for (int nj = 0; nj < 7; ++nj) {
          u32x2 o = {pack2(acc[mi][nj][0], acc[mi][nj][1]), pack2(acc[mi][nj][2], acc[mi][nj][3])};
          *(u32x2*)(st + (16 * nj + fr) * 272 + (wave * 32 + 16 * mi + 4 * fq) * 2) = o;
        }
    }
    lds_barrier();
    float sum[8], sq[8];
#pragma unroll
    for (int e = 0; e < 8; ++e) { sum[e] = 0.f; sq[e] = 0.f; }
    const int sc = t & 15;
#pragma unroll
    for (int i = 0; i < 4; ++i) {
      const int fl = (t >> 4) + 32 * i;
      if (fl < 112) {
        u32x4 w = *(const u32x4*)(st + fl * 272 + sc * 16);
        u32x4 o;
#pragma unroll
        for (int e = 0; e < 4; ++e) o[e] = pack2(gelu_fast(lo2f(w[e])), gelu_fast(hi2f(w[e])));
        *(u32x4*)(z2T + (size_t)(f0 + fl) * S_ + m0 + sc * 8) = o;
        if (f0 + fl < FH) {
#pragma unroll
          for (int e = 0; e < 4; ++e) {
            float a = lo2f(o[e]), bq = hi2f(o[e]);
            sum[2 * e] += a; sq[2 * e] += a * a;
            sum[2 * e + 1] += bq; sq[2 * e + 1] += bq * bq;
          }
        }
      }
    }
#pragma unroll
    for (int e = 0; e < 8; ++e) {
      sum[e] += __shfl_xor(sum[e], 16); sum[e] += __shfl_xor(sum[e], 32);
      sq[e] += __shfl_xor(sq[e], 16); sq[e] += __shfl_xor(sq[e], 32);
    }
    float2* red = (float2*)(lds + (11 % G1_NS) * (240 * 128));
    if (lane < 16) {
#pragma unroll
      for (int e = 0; e < 8; ++e) red[wave * 128 + sc * 8 + e] = make_float2(sum[e], sq[e]);
    }
    lds_barrier();
    if (t < 128) {
      float a = 0.f, q = 0.f;
#pragma unroll
      for (int w = 0; w < 8; ++w) { float2 v = red[w * 128 + t]; a += v.x; q += v.y; }
      part[(size_t)nt2 * S_ + m0 + t] = make_float2(a, q);
    }
  }
}

__global__ __launch_bounds__(256) void norm_kernel(const u16* __restrict__ z2T, const float2* __restrict__ part,
                                                   u16* __restrict__ z2nT) {
  __shared__ float sm[256], si[256];
  const int t = threadIdx.x;
  const int s0 = (blockIdx.x & 7) * 256, f0 = (blockIdx.x >> 3) * 32;
  {
    float sum = 0.f, sq = 0.f;
#pragma unroll
    for (int j = 0; j < 8; ++j) { float2 p = part[(size_t)j * S_ + s0 + t]; sum += p.x; sq += p.y; }
    float mean = sum * (1.0f / FH);
    float var = fmaxf(sq * (1.0f / FH) - mean * mean, 0.f);
    sm[t] = rbf(mean);
    si[t] = 1.0f / rbf(sqrtf(rbf(rbf(var) + 1e-5f)));
  }
  __syncthreads();
  const int sc = t & 31, fr = t >> 5;
  float mm[8], ii[8];
#pragma unroll
  for (int e = 0; e < 8; ++e) { mm[e] = sm[sc * 8 + e]; ii[e] = si[sc * 8 + e]; }
#pragma unroll
  for (int h = 0; h < 4; ++h) {
    const int f = f0 + fr + 8 * h;
    u32x4 w = *(const u32x4*)(z2T + (size_t)f * S_ + s0 + sc * 8);
    u32x4 o;
#pragma unroll
    for (int e = 0; e < 4; ++e)
      o[e] = pack2(rbf(lo2f(w[e]) - mm[2 * e]) * ii[2 * e], rbf(hi2f(w[e]) - mm[2 * e + 1]) * ii[2 * e + 1]);
    *(u32x4*)(z2nT + (size_t)f * S_ + s0 + sc * 8) = o;
  }
}

constexpr int G2_NS = 5;
__global__ __launch_bounds__(512) void gemm2_kernel(const u16* __restrict__ w_sp, const u16* __restrict__ z2nT,
                                                    const u16* __restrict__ b_sp, const u16* __restrict__ z1,
                                                    u16* __restrict__ u) {
  __shared__ __attribute__((aligned(16))) char lds[G2_NS * 176 * 128];
  const int b = blockIdx.x, xcd = b & 7, idx = b >> 3;
  const int mt = (xcd >> 1) * 8 + (idx & 7);
  const int nt = (xcd & 1) * 4 + (idx >> 3);
  const int m0 = mt * 64, n0 = nt * 112;
  const int t = threadIdx.x, lane = t & 63, wave = t >> 6;
  const int fr = lane & 15, fq = lane >> 4;
  f32x4 acc[1][7];
#pragma unroll
  for (int j = 0; j < 7; ++j) acc[0][j] = (f32x4){0.f, 0.f, 0.f, 0.f};
  u32x4 zr[4];
  char* st = lds + (32 % G2_NS) * (176 * 128);
  float bias = 0.f;
  if (wave < 4) bias = bf2f(b_sp[m0 + wave * 16 + fr]);
  gemm_core<64, 112, G2_NS, 32, true, 2, 4>(w_sp + (size_t)m0 * S_, S_, z2nT + (size_t)n0 * S_, S_, lds, acc, [&] {
#pragma unroll
    for (int i = 0; i < 4; ++i) {
      int c = (t - 256) + 256 * i; if (c > 895) c = 895;
      const int row = c / 14, ch = c - row * 14;
      zr[i] = *(const u32x4*)(z1 + (size_t)(m0 + row) * FP + n0 + ch * 8);
    }
  });
  if (wave < 4) {
#pragma unroll
    for (int nj = 0; nj < 7; ++nj) {
      float g0 = rbf(acc[0][nj][0]) + bias, g1 = rbf(acc[0][nj][1]) + bias;
      float g2 = rbf(acc[0][nj][2]) + bias, g3 = rbf(acc[0][nj][3]) + bias;
      u32x2 o = {pack2(g0, g1), pack2(g2, g3)};
      *(u32x2*)(st + (wave * 16 + fr) * 240 + nj * 32 + fq * 8) = o;
    }
    lds_barrier();
  } else {
    lds_barrier();
#pragma unroll
    for (int i = 0; i < 4; ++i) {
      const int c = (t - 256) + 256 * i;
      if (c < 896) {
        const int row = c / 14, ch = c - row * 14;
        u32x4 g = *(const u32x4*)(st + row * 240 + ch * 16);
        u32x4 o;
#pragma unroll
        for (int e = 0; e < 4; ++e) o[e] = pack2(lo2f(zr[i][e]) * lo2f(g[e]), hi2f(zr[i][e]) * hi2f(g[e]));
        *(u32x4*)(u + (size_t)(m0 + row) * FP + n0 + ch * 8) = o;
      }
    }
  }
}

constexpr int G3_NS = 6;
__global__ __launch_bounds__(512) void gemm3_kernel(const u16* __restrict__ u, const u16* __restrict__ w_outT,
                                                    const u16* __restrict__ xres, u16* __restrict__ out) {
  __shared__ __attribute__((aligned(16))) char lds[G3_NS * 144 * 128];
  const int b = blockIdx.x, xcd = b & 7, idx = b >> 3;
  const int mt = (xcd >> 1) * 8 + (idx & 7);
  const int nt = (xcd & 1) * 4 + (idx >> 3);
  const int m0 = mt * 64, n0 = nt * 80;
  const int t = threadIdx.x, lane = t & 63, wave = t >> 6;
  const int fr = lane & 15, fq = lane >> 4;
  f32x4 acc[1][5];
#pragma unroll
  for (int j = 0; j < 5; ++j) acc[0][j] = (f32x4){0.f, 0.f, 0.f, 0.f};
  u32x4 xr[3];
  char* st = lds + (14 % G3_NS) * (144 * 128);
  gemm_core<64, 80, G3_NS, 14, true, 3, 3>(u + (size_t)m0 * FP, FP, w_outT + (size_t)n0 * FP, FP, lds, acc, [&] {
#pragma unroll
    for (int i = 0; i < 3; ++i) {
      int c = (t - 256) + 256 * i; if (c > 639) c = 639;
      const int row = c / 10, ch = c - row * 10;
      xr[i] = *(const u32x4*)(xres + (size_t)(m0 + row) * D_ + n0 + ch * 8);
    }
  });
  if (wave < 4) {
#pragma unroll
    for (int nj = 0; nj < 5; ++nj) {
      u32x2 o = {pack2(acc[0][nj][0], acc[0][nj][1]), pack2(acc[0][nj][2], acc[0][nj][3])};
      *(u32x2*)(st + (wave * 16 + fr) * 176 + nj * 32 + fq * 8) = o;
    }
    lds_barrier();
  } else {
    lds_barrier();
#pragma unroll
    for (int i = 0; i < 3; ++i) {
      const int c = (t - 256) + 256 * i;
      if (c < 640) {
        const int row = c / 10, ch = c - row * 10;
        u32x4 g = *(const u32x4*)(st + row * 176 + ch * 16);
        u32x4 o;
#pragma unroll
        for (int e = 0; e < 4; ++e) o[e] = pack2(lo2f(xr[i][e]) + lo2f(g[e]), hi2f(xr[i][e]) + hi2f(g[e]));
        *(u32x4*)(out + (size_t)(m0 + row) * D_ + n0 + ch * 8) = o;
      }
    }
  }
}

extern "C" void kernel_launch(void* const* d_in, const int* in_sizes, int n_in, void* d_out, int out_size,
                              void* d_ws, size_t ws_size, hipStream_t stream) {
  const u16* A_q = (const u16*)d_in[0];
  const u16* A_k = (const u16*)d_in[1];
  const u16* A_v = (const u16*)d_in[2];
  const u16* A_shift_k = (const u16*)d_in[3];
  const u16* A_ssm_k = (const u16*)d_in[4];
  const u16* A_ssm_D = (const u16*)d_in[5];
  const u16* B_w_in = (const u16*)d_in[7];
  const u16* B_w_sp = (const u16*)d_in[8];
  const u16* B_b_sp = (const u16*)d_in[9];
  const u16* B_w_out = (const u16*)d_in[10];
  u16* out = (u16*)d_out;

  char* ws = (char*)d_ws;
  size_t off = 0;
  auto take = [&](size_t bytes) { char* p = ws + off; off += (bytes + 255) & ~(size_t)255; return p; };
  u16* kvT = (u16*)take((size_t)D_ * S_ * 2);
  u16* YT = (u16*)take((size_t)D_ * S_ * 2);
  u16* w_inT = (u16*)take((size_t)2 * FP * D_ * 2);
  u16* w_outT = (u16*)take((size_t)D_ * FP * 2);
  u16* xa = (u16*)take((size_t)S_ * D_ * 2);
  u16* xb = (u16*)take((size_t)S_ * D_ * 2);
  u16* xn = (u16*)take((size_t)S_ * D_ * 2);
  u16* z1 = (u16*)take((size_t)S_ * FP * 2);
  u16* z2T = (u16*)take((size_t)FP * S_ * 2);
  u16* z2nT = (u16*)take((size_t)FP * S_ * 2);
  u16* u = (u16*)take((size_t)S_ * FP * 2);
  float2* part = (float2*)take((size_t)8 * S_ * 8);

  pre_kernel<<<PRE_KV_BLOCKS + PRE_WIN_BLOCKS + PRE_WOUT_BLOCKS + PRE_PAD_BLOCKS, 256, 0, stream>>>(
      A_k, A_v, A_shift_k, B_w_in, B_w_out, kvT, w_inT, w_outT);
  conv_kernel<<<640, 256, 0, stream>>>(kvT, A_ssm_k, A_ssm_D, YT);
  rowop_kernel<<<256, 512, 0, stream>>>(A_q, YT, xa, xn);
  gemm1_kernel<<<256, 512, 0, stream>>>(xn, w_inT, z1, z2T, part);
  norm_kernel<<<224, 256, 0, stream>>>(z2T, part, z2nT);
  gemm2_kernel<<<256, 512, 0, stream>>>(B_w_sp, z2nT, B_b_sp, z1, u);
  gemm3_kernel<<<256, 512, 0, stream>>>(u, w_outT, xa, xb);
  rowop_kernel<<<256, 512, 0, stream>>>(xb, YT, xa, xn);
  gemm1_kernel<<<256, 512, 0, stream>>>(xn, w_inT, z1, z2T, part);
  norm_kernel<<<224, 256, 0, stream>>>(z2T, part, z2nT);
  gemm2_kernel<<<256, 512, 0, stream>>>(B_w_sp, z2nT, B_b_sp, z1, u);
  gemm3_kernel<<<256, 512, 0, stream>>>(u, w_outT, xa, out);
}
```

```cpp
#include <hip/hip_runtime.h>
#include <hip/hip_bf16.h>
#include <stdint.h>

typedef unsigned short u16;
typedef unsigned int u32;
typedef __attribute__((ext_vector_type(8))) short bf16x8;
typedef __attribute__((ext_vector_type(4))) float f32x4;
typedef __attribute__((ext_vector_type(4))) unsigned int u32x4;
typedef __attribute__((ext_vector_type(2))) unsigned int u32x2;

#define DEV __device__ __forceinline__

constexpr int S_ = 2048, D_ = 640, FH = 864, FP = 896;

DEV float bf2f(u16 v) { return __uint_as_float(((u32)v) << 16); }
DEV float lo2f(u32 v) { return __uint_as_float(v << 16); }
DEV float hi2f(u32 v) { return __uint_as_float(v & 0xffff0000u); }
DEV float rbf(float f) { return (float)(__bf16)f; }
DEV u16 f2bf(float f) { __bf16 b = (__bf16)f; return __builtin_bit_cast(u16, b); }
DEV u32 pack2(float a, float b) { return (u32)f2bf(a) | ((u32)f2bf(b) << 16); }

DEV float wave_sum(float v) {
#pragma unroll
  for (int o = 32; o > 0; o >>= 1) v += __shfl_xor(v, o);
  return v;
}

DEV float gelu_tanh(float x) {
  float y = 0.7978845608028654f * (x + 0.044715f * x * x * x);
  float e = __expf(-2.0f * y);
  return __fdividef(x, 1.0f + e);
}

constexpr int PRE_KV_BLOCKS = 320, PRE_TOUCH_BLOCKS = 64;
constexpr int AUX_WIN_BLOCKS = 270, AUX_WOUT_BLOCKS = 140, AUX_PAD_BLOCKS = 4;
constexpr int AUX_BLOCKS = AUX_WIN_BLOCKS + AUX_WOUT_BLOCKS + AUX_PAD_BLOCKS;

DEV void touch(const void* p, int n16, int blk, int nblk) {
  const u32x4* q = (const u32x4*)p;
  const int per = (n16 + nblk - 1) / nblk;
  const int lo = blk * per, hi = min(lo + per, n16);
  for (int i = lo + (int)threadIdx.x; i < hi; i += 256 * 8) {
    u32x4 w[8];
#pragma unroll
    for (int j = 0; j < 8; ++j) { int c = i + 256 * j; w[j] = q[c < hi ? c : i]; }
#pragma unroll
    for (int j = 0; j < 8; ++j) asm volatile("" ::"v"(w[j]));
  }
}

DEV void aux_work(int b, u16 (*tile)[72], const u16* __restrict__ w_in, const u16* __restrict__ w_out,
                  u16* __restrict__ w_inT, u16* __restrict__ w_outT) {
  const int t = threadIdx.x;
  if (b < AUX_WIN_BLOCKS + AUX_WOUT_BLOCKS) {
    const u16* src; u16* dst; int ld_src, ld_dst, r0, c0, rmax, drow0;
    if (b < AUX_WIN_BLOCKS) {
      int kt = b % 10, nt = b / 10;
      src = w_in; ld_src = 2 * FH; r0 = kt * 64; c0 = nt * 64; rmax = D_;
      dst = w_inT; ld_dst = D_; drow0 = 0;
    } else {
      int bb = b - AUX_WIN_BLOCKS;
      int kt = bb % 14, dt = bb / 14;
      src = w_out; ld_src = D_; r0 = kt * 64; c0 = dt * 64; rmax = FH;
      dst = w_outT; ld_dst = FP; drow0 = 1;
    }
#pragma unroll
    for (int h = 0; h < 2; ++h) {
      int idx = t + 256 * h;
      int r = idx >> 3, ch = idx & 7;
      u32x4 w = (u32x4){0u, 0u, 0u, 0u};
      if (r0 + r < rmax) w = *(const u32x4*)(src + (size_t)(r0 + r) * ld_src + c0 + ch * 8);
#pragma unroll
      for (int e = 0; e < 4; ++e) {
        tile[ch * 8 + 2 * e][r] = (u16)(w[e] & 0xffffu);
        tile[ch * 8 + 2 * e + 1][r] = (u16)(w[e] >> 16);
      }
    }
    __syncthreads();
#pragma unroll
    for (int h = 0; h < 2; ++h) {
      int idx = t + 256 * h;
      int cr = idx >> 3, ch = idx & 7;
      int n = c0 + cr;
      int drow = drow0 ? n : (n < FH ? n : n + 32);
      u32x4 o = *(const u32x4*)(&tile[cr][ch * 8]);
      *(u32x4*)(dst + (size_t)drow * ld_dst + r0 + ch * 8) = o;
    }
    return;
  }
  b -= AUX_WIN_BLOCKS + AUX_WOUT_BLOCKS;
  if (b < AUX_PAD_BLOCKS) {
    for (int i = b * 256 + t; i < 5120; i += AUX_PAD_BLOCKS * 256) {
      int row = i / 80, ch = i % 80;
      int rr = row < 32 ? FH + row : FP + FH + (row - 32);
      *(u32x4*)(w_inT + (size_t)rr * D_ + ch * 8) = (u32x4){0u, 0u, 0u, 0u};
    }
    return;
  }
}

__global__ __launch_bounds__(256) void pre_kernel(const u16* __restrict__ k, const u16* __restrict__ v,
                                                  const u16* __restrict__ shift_k, const u16* __restrict__ ssm_k,
                                                  const u16* __restrict__ w_in, const u16* __restrict__ w_out,
                                                  u16* __restrict__ kvT, u16* __restrict__ w_inT,
                                                  u16* __restrict__ w_outT, u32x4* __restrict__ part) {
  __shared__ u16 tile[64][72];
  const int t = threadIdx.x;
  int b = blockIdx.x;
  if (b < PRE_KV_BLOCKS) {
    const int s0 = (b & 31) * 64, d0 = (b >> 5) * 64;
    const int c8 = t & 7, r0 = t >> 3;
    const int d = d0 + c8 * 8;
    float sk[8][4];
#pragma unroll
    for (int j = 0; j < 4; ++j) {
      u32x4 w = *(const u32x4*)(shift_k + (d + 2 * j) * 4);
      sk[2 * j][0] = lo2f(w[0]); sk[2 * j][1] = hi2f(w[0]); sk[2 * j][2] = lo2f(w[1]); sk[2 * j][3] = hi2f(w[1]);
      sk[2 * j + 1][0] = lo2f(w[2]); sk[2 * j + 1][1] = hi2f(w[2]); sk[2 * j + 1][2] = lo2f(w[3]); sk[2 * j + 1][3] = hi2f(w[3]);
    }
#pragma unroll
    for (int h = 0; h < 2; ++h) {
      const int r = r0 + 32 * h;
      const int s = s0 + r;
      u32x4 kk[4];
#pragma unroll
      for (int j = 0; j < 4; ++j) {
        if (s - j >= 0) kk[j] = *(const u32x4*)(k + (size_t)(s - j) * D_ + d);
        else kk[j] = (u32x4){0u, 0u, 0u, 0u};
      }
      u32x4 vv = *(const u32x4*)(v + (size_t)s * D_ + d);
#pragma unroll
      for (int e = 0; e < 8; ++e) {
        float acc = 0.f;
#pragma unroll
        for (int tt = 0; tt < 4; ++tt) {
          u32 w = kk[3 - tt][e >> 1];
          float kf = (e & 1) ? hi2f(w) : lo2f(w);
          float p = rbf(kf * sk[e][3 - tt]);
          acc = rbf(acc + p);
        }
        u32 w = vv[e >> 1];
        float vf = (e & 1) ? hi2f(w) : lo2f(w);
        tile[c8 * 8 + e][r] = f2bf(acc * vf);
      }
    }
    __syncthreads();
#pragma unroll
    for (int h = 0; h < 2; ++h) {
      int idx = t + 256 * h;
      int dr = idx >> 3, ch = idx & 7;
      u32x4 o = *(const u32x4*)(&tile[dr][ch * 8]);
      *(u32x4*)(kvT + (size_t)(d0 + dr) * S_ + s0 + ch * 8) = o;
    }
    return;
  }
  b -= PRE_KV_BLOCKS;
  if (b < AUX_BLOCKS) { aux_work(b, tile, w_in, w_out, w_inT, w_outT); return; }
  b -= AUX_BLOCKS;
  part[b * 256 + t] = (u32x4){0xffffffffu, 0xffffffffu, 0xffffffffu, 0xffffffffu};
  touch(ssm_k, D_ * S_ / 8, b, PRE_TOUCH_BLOCKS);
}

constexpr int CV_E_BYTES = 2448 * 16;
constexpr int CV_X_BYTES = 32 * 272;

__global__ __launch_bounds__(256) void conv_kernel(const u16* __restrict__ kvT, const u16* __restrict__ ssm_k,
                                                   const u16* __restrict__ ssm_D, u16* __restrict__ YT,
                                                   const u16* __restrict__ w_sp, const u16* __restrict__ q) {
  __shared__ __attribute__((aligned(16))) char smem[CV_E_BYTES + CV_X_BYTES];
  char* Et = smem;
  char* Xp = smem + CV_E_BYTES;
  const int t = threadIdx.x, lane = t & 63;
  const int wave = __builtin_amdgcn_readfirstlane(t >> 6);
  const int d = blockIdx.x;
  const u16* h = ssm_k + (size_t)d * S_;
  const u16* x = kvT + (size_t)d * S_;
  {
    u32x4 w1 = *(const u32x4*)(h + 8 * t);
    u32x4 w0 = (u32x4){0u, 0u, 0u, 0u};
    if (t > 0) w0 = *(const u32x4*)(h + 8 * t - 8);
    u32 w[8] = {w0[0], w0[1], w0[2], w0[3], w1[0], w1[1], w1[2], w1[3]};
    char* base = Et + (144 + 9 * t) * 16;
#pragma unroll
    for (int r = 0; r < 8; ++r) {
      u32x4 g;
#pragma unroll
      for (int dd = 0; dd < 4; ++dd) {
        if ((r & 1) == 0) {
          int p = (8 + r - 2 * dd) >> 1;
          g[dd] = (w[p] & 0xffffu) | (w[p - 1] & 0xffff0000u);
        } else {
          int p = (8 + r - 2 * dd - 1) >> 1;
          g[dd] = (w[p] >> 16) | (w[p] << 16);
        }
      }
      *(u32x4*)(base + r * 16) = g;
    }
    if (t < 144) *(u32x4*)(Et + t * 16) = (u32x4){0u, 0u, 0u, 0u};
    u32x4 xv = *(const u32x4*)(x + 8 * t);
    *(u32x4*)(Xp + (16 + (t >> 4)) * 272 + (t & 15) * 16) = xv;
    for (int i = t; i < 16 * 17; i += 256) *(u32x4*)(Xp + i * 16) = (u32x4){0u, 0u, 0u, 0u};
  }
  __syncthreads();
  u32x4 pf[5];
  {
    const int base = d * 256 + t;
#pragma unroll
    for (int j = 0; j < 4; ++j) {
      int c = base + j * (D_ * 256); if (c >= S_ * S_ / 8) c = base;
      pf[j] = ((const u32x4*)w_sp)[c];
    }
    pf[4] = ((const u32x4*)q)[base];
  }
  const int p = wave & 1, hk = wave >> 1;
  const int m = lane & 15, kg = lane >> 4;
  const char* abase = Et + (m + (m >> 3) - 9 * kg) * 16 + 288 * p + 2304;
  const char* bbase = Xp + (m + 16) * 272 + kg * 16;
  f32x4 acc[4];
#pragma unroll
  for (int i = 0; i < 4; ++i) acc[i] = (f32x4){0.f, 0.f, 0.f, 0.f};
  bf16x8 F[4];
  const int D0 = 8 * hk;
  F[1] = *(const bf16x8*)(abase + 576 * (4 * D0 - 3));
  F[2] = *(const bf16x8*)(abase + 576 * (4 * D0 - 2));
  F[3] = *(const bf16x8*)(abase + 576 * (4 * D0 - 1));
  for (int dl = 0; dl < 8; ++dl) {
    const int Dl = D0 + dl;
    const char* ab = abase + 576 * 4 * Dl;
    const char* bb = bbase - 272 * Dl;
#pragma unroll
    for (int qq = 0; qq < 4; ++qq) {
      const int q = 3 - qq;
      F[qq] = *(const bf16x8*)(ab + 576 * qq);
      bf16x8 B = *(const bf16x8*)(bb + 64 * q);
#pragma unroll
      for (int i = 0; i < 4; ++i) {
        acc[i] = __builtin_amdgcn_mfma_f32_16x16x32_bf16(F[(i - q) & 3], B, acc[i], 0, 0, 0);
      }
    }
  }
  __syncthreads();
  float* yp = (float*)Et;
  u16* orow = (u16*)(Et + 8704);
  const int a = m, g = kg;
  if (hk == 1) {
#pragma unroll
    for (int i = 0; i < 4; ++i) {
      int mi = p + 2 * i;
      *(f32x4*)(yp + a * 132 + 16 * mi + 4 * g) = acc[i];
    }
  }
  __syncthreads();
  if (hk == 0) {
    const float Dv = bf2f(ssm_D[d]);
#pragma unroll
    for (int i = 0; i < 4; ++i) {
      int mi = p + 2 * i;
      f32x4 o = *(const f32x4*)(yp + a * 132 + 16 * mi + 4 * g);
      u32x2 xv = *(const u32x2*)(Xp + (a + 16) * 272 + (16 * mi + 4 * g) * 2);
      float xs[4] = {lo2f(xv[0]), hi2f(xv[0]), lo2f(xv[1]), hi2f(xv[1])};
      float res[4];
#pragma unroll
      for (int r = 0; r < 4; ++r) {
        float y = rbf(acc[i][r] + o[r]);
        float kd = rbf(xs[r] * Dv);
        res[r] = y + kd;
      }
      u32x2 ov = {pack2(res[0], res[1]), pack2(res[2], res[3])};
      *(u32x2*)(orow + 128 * a + 16 * mi + 4 * g) = ov;
    }
  }
  __syncthreads();
  *(u32x4*)(YT + (size_t)d * S_ + 8 * t) = *(const u32x4*)(orow + 8 * t);
#pragma unroll
  for (int j = 0; j < 5; ++j) asm volatile("" ::"v"(pf[j]));
}

DEV void ln10(const float (&v)[10], float (&o)[10]) {
  float s = 0.f;
#pragma unroll
  for (int i = 0; i < 10; ++i) s += v[i];
  s = wave_sum(s);
  float mean = s * (1.0f / 640.0f);
  float q = 0.f;
#pragma unroll
  for (int i = 0; i < 10; ++i) { float dd = v[i] - mean; q += dd * dd; }
  q = wave_sum(q);
  float var = rbf(q * (1.0f / 640.0f));
  float mb = rbf(mean);
  float den = rbf(sqrtf(rbf(var + 1e-5f)));
  float inv = 1.0f / den;
#pragma unroll
  for (int i = 0; i < 10; ++i) o[i] = rbf(rbf(v[i] - mb) * inv);
}

__global__ __launch_bounds__(512) void rowop_kernel(const u16* __restrict__ xin, const u16* __restrict__ YT,
                                                    u16* __restrict__ xo, u16* __restrict__ xn) {
  __shared__ u16 yt[8][648];
  const int t = threadIdx.x, lane = t & 63, r = t >> 6;
  const int s0 = ((blockIdx.x & 7) * 32 + (blockIdx.x >> 3)) * 8;
  for (int d = t; d < D_; d += 512) {
    u32x4 w = *(const u32x4*)(YT + (size_t)d * S_ + s0);
#pragma unroll
    for (int e = 0; e < 4; ++e) {
      yt[2 * e][d] = (u16)(w[e] & 0xffffu);
      yt[2 * e + 1][d] = (u16)(w[e] >> 16);
    }
  }
  float v[10];
  const u16* xr = xin + (size_t)(s0 + r) * D_;
#pragma unroll
  for (int i = 0; i < 5; ++i) {
    u32 w = *(const u32*)(xr + 2 * lane + 128 * i);
    v[2 * i] = lo2f(w); v[2 * i + 1] = hi2f(w);
  }
  float a[10];
  ln10(v, a);
  __syncthreads();
  float x1[10];
#pragma unroll
  for (int i = 0; i < 5; ++i) {
    u32 w = *(const u32*)(&yt[r][2 * lane + 128 * i]);
    x1[2 * i] = rbf(v[2 * i] + rbf(a[2 * i] * lo2f(w)));
    x1[2 * i + 1] = rbf(v[2 * i + 1] + rbf(a[2 * i + 1] * hi2f(w)));
  }
  float n1[10];
  ln10(x1, n1);
  u16* xor_ = xo + (size_t)(s0 + r) * D_;
  u16* xnr = xn + (size_t)(s0 + r) * D_;
#pragma unroll
  for (int i = 0; i < 5; ++i) {
    *(u32*)(xor_ + 2 * lane + 128 * i) = pack2(x1[2 * i], x1[2 * i + 1]);
    *(u32*)(xnr + 2 * lane + 128 * i) = pack2(n1[2 * i], n1[2 * i + 1]);
  }
}

template <int N> DEV void wait_vm() {
  asm volatile("s_waitcnt vmcnt(%0)" ::"n"(N) : "memory");
}

template <int BM, int BN, int NS, int NK, bool SWAP, int KID, int EXTRA, class PRE>
DEV void gemm_core(const u16* __restrict__ A, int lda, const u16* __restrict__ Bt, int ldb, char* lds,
                   f32x4 (&acc)[BM / 64][BN / 16], PRE&& pre) {
  constexpr int T = (BM + BN) / 8;
  constexpr int NI = (T + 3) / 4;
  constexpr int REM = T % 4;
  constexpr int STAGE = (BM + BN) * 128;
  constexpr int MI = BM / 64, NJ = BN / 16;
  const int t = threadIdx.x, lane = t & 63;
  const int wave = __builtin_amdgcn_readfirstlane(t >> 6);
  if (wave >= 4) {
    const int lw = wave - 4;
    const u16* src[NI];
#pragma unroll
    for (int j = 0; j < NI; ++j) {
      int i = lw + 4 * j;
      if (i >= T) i = T - 1;
      int row = 8 * i + (lane >> 3);
      int cg = (lane & 7) ^ (row & 7);
      src[j] = (8 * i < BM) ? (A + (size_t)row * lda + cg * 8) : (Bt + (size_t)(row - BM) * ldb + cg * 8);
    }
    const bool full = (REM == 0) || (lw < REM);
    auto issue = [&](int kt) {
      char* dst = lds + (kt % NS) * STAGE + lw * 1024;
#pragma unroll
      for (int j = 0; j < NI; ++j) {
        if (j < NI - 1 || full)
          __builtin_amdgcn_global_load_lds((const u32*)(src[j] + kt * 64), (u32*)(dst + j * 4096), 16, 0, 0);
      }
    };
    auto waitfor = [&](int rem) {
      if (rem <= 0) wait_vm<0>();
      else if (rem == 1) { if (full) wait_vm<NI>(); else wait_vm<NI - 1>(); }
      else if (rem == 2) { if (full) wait_vm<2 * NI>(); else wait_vm<2 * (NI - 1)>(); }
      else if (rem == 3) { if (full) wait_vm<3 * NI>(); else wait_vm<3 * (NI - 1)>(); }
      else if (rem == 4) { if (full) wait_vm<4 * NI>(); else wait_vm<4 * (NI - 1)>(); }
      else if (rem == 5) { if (full) wait_vm<5 * NI>(); else wait_vm<5 * (NI - 1)>(); }
      else wait_vm<0>();
    };
    auto waitforx = [&](int rem) {
      if (rem <= 0) wait_vm<EXTRA>();
      else if (rem == 1) { if (full) wait_vm<EXTRA + NI>(); else wait_vm<EXTRA + NI - 1>(); }
      else if (rem == 2) { if (full) wait_vm<EXTRA + 2 * NI>(); else wait_vm<EXTRA + 2 * (NI - 1)>(); }
      else if (rem == 3) { if (full) wait_vm<EXTRA + 3 * NI>(); else wait_vm<EXTRA + 3 * (NI - 1)>(); }
      else if (rem == 4) { if (full) wait_vm<EXTRA + 4 * NI>(); else wait_vm<EXTRA + 4 * (NI - 1)>(); }
      else if (rem == 5) { if (full) wait_vm<EXTRA + 5 * NI>(); else wait_vm<EXTRA + 5 * (NI - 1)>(); }
      else wait_vm<0>();
    };
    static_assert(NS - 2 <= 5, "");
#pragma unroll
    for (int s = 0; s < NS - 1; ++s) issue(s);
    constexpr int MAINK = NK - (NS - 1);
    for (int kt = 0; kt < MAINK; ++kt) {
      waitfor(NS - 2);
      __builtin_amdgcn_s_barrier();
      issue(kt + NS - 1);
    }
    pre();
#pragma unroll
    for (int kt = MAINK; kt < NK; ++kt) {
      waitforx(NK - 1 - kt);
      __builtin_amdgcn_s_barrier();
    }
    return;
  }
  const int fr = lane & 15, fq = lane >> 4;
  int aoff[MI], boff[NJ];
#pragma unroll
  for (int mi = 0; mi < MI; ++mi) aoff[mi] = (wave * (BM / 4) + 16 * mi + fr) * 128;
#pragma unroll
  for (int nj = 0; nj < NJ; ++nj) boff[nj] = (BM + 16 * nj + fr) * 128;
  const int sw = fr & 7;
  bf16x8 fa[2][2][MI], fb[2][2][NJ];
  auto rd = [&](int kt, bf16x8 (&a)[2][MI], bf16x8 (&bb)[2][NJ]) {
    const char* base = lds + (kt % NS) * STAGE;
#pragma unroll
    for (int kk = 0; kk < 2; ++kk) {
      const int ch = ((4 * kk + fq) ^ sw) * 16;
#pragma unroll
      for (int mi = 0; mi < MI; ++mi) a[kk][mi] = *(const bf16x8*)(base + aoff[mi] + ch);
#pragma unroll
      for (int nj = 0; nj < NJ; ++nj) bb[kk][nj] = *(const bf16x8*)(base + boff[nj] + ch);
    }
  };
  auto mma = [&](bf16x8 (&a)[2][MI], bf16x8 (&bb)[2][NJ]) {
#pragma unroll
    for (int kk = 0; kk < 2; ++kk)
#pragma unroll
      for (int mi = 0; mi < MI; ++mi)
#pragma unroll
        for (int nj = 0; nj < NJ; ++nj) {
          if (SWAP) acc[mi][nj] = __builtin_amdgcn_mfma_f32_16x16x32_bf16(bb[kk][nj], a[kk][mi], acc[mi][nj], 0, 0, 0);
          else acc[mi][nj] = __builtin_amdgcn_mfma_f32_16x16x32_bf16(a[kk][mi], bb[kk][nj], acc[mi][nj], 0, 0, 0);
        }
  };
  static_assert(NK % 2 == 0, "");
  auto ilv = [&]() {
    constexpr int NR = 2 * (MI + NJ), NM = 2 * MI * NJ;
    if constexpr (NM >= NR) {
      constexpr int PER = NM / NR;
#pragma unroll
      for (int i = 0; i < NR; ++i) {
        __builtin_amdgcn_sched_group_barrier(0x100, 1, 0);
        __builtin_amdgcn_sched_group_barrier(0x008, PER, 0);
      }
      __builtin_amdgcn_sched_group_barrier(0x008, NM - PER * NR, 0);
    } else {
#pragma unroll
      for (int i = 0; i < NM; ++i) {
        __builtin_amdgcn_sched_group_barrier(0x100, 1, 0);
        __builtin_amdgcn_sched_group_barrier(0x008, 1, 0);
      }
      __builtin_amdgcn_sched_group_barrier(0x100, NR - NM, 0);
    }
  };
  auto lgk0 = [&]() { __builtin_amdgcn_sched_barrier(0); asm volatile("s_waitcnt lgkmcnt(0)" ::: "memory"); };
  __builtin_amdgcn_s_barrier();
  rd(0, fa[0], fb[0]);
  __builtin_amdgcn_sched_barrier(0);
  lgk0();
  __builtin_amdgcn_s_barrier();
  rd(1, fa[1], fb[1]);
  __builtin_amdgcn_sched_barrier(0);
  mma(fa[0], fb[0]);
  for (int kt = 2; kt < NK; kt += 2) {
    lgk0();
    __builtin_amdgcn_s_barrier();
    rd(kt, fa[0], fb[0]);
    mma(fa[1], fb[1]);
    ilv();
    lgk0();
    __builtin_amdgcn_s_barrier();
    rd(kt + 1, fa[1], fb[1]);
    mma(fa[0], fb[0]);
    ilv();
  }
  mma(fa[1], fb[1]);
}

DEV float gelu_fast(float x) {
  const float c1 = -2.0f * 0.7978845608028654f * 1.4426950408889634f;
  const float c2 = c1 * 0.044715f;
  float e = __builtin_amdgcn_exp2f(x * __builtin_fmaf(x * x, c2, c1));
  return x * __builtin_amdgcn_rcpf(1.0f + e);
}
DEV void lds_barrier() { asm volatile("s_waitcnt lgkmcnt(0)" ::: "memory"); __builtin_amdgcn_s_barrier(); asm volatile("" ::: "memory"); }

constexpr int G1_NS = 4;
__global__ __launch_bounds__(512) void gemm1_kernel(const u16* __restrict__ xn, const u16* __restrict__ w_inT,
                                                    u16* __restrict__ z1, u16* __restrict__ z2nT,
                                                    unsigned long long* __restrict__ part) {
  __shared__ __attribute__((aligned(16))) char lds[G1_NS * 240 * 128];
  const int b = blockIdx.x, xcd = b & 7, idx = b >> 3;
  const int mt = (xcd >> 1) * 4 + (idx & 3);
  const int nt = (xcd & 1) * 8 + (idx >> 2);
  const int m0 = mt * 128, n0 = nt * 112;
  const int t = threadIdx.x, lane = t & 63, wave = t >> 6;
  const int fr = lane & 15, fq = lane >> 4;
  f32x4 acc[2][7];
#pragma unroll
  for (int i = 0; i < 2; ++i)
#pragma unroll
    for (int j = 0; j < 7; ++j) acc[i][j] = (f32x4){0.f, 0.f, 0.f, 0.f};
  char* st = lds + (10 % G1_NS) * (240 * 128);
  if (nt < 8) {
    gemm_core<128, 112, G1_NS, 10, true, 1, 0>(xn + (size_t)m0 * D_, D_, w_inT + (size_t)n0 * D_, D_, lds, acc, [] {});
    if (wave < 4) {
#pragma unroll
      for (int mi = 0; mi < 2; ++mi)
#pragma unroll
        for (int nj = 0; nj < 7; ++nj) {
          u32x2 o = {pack2(acc[mi][nj][0], acc[mi][nj][1]), pack2(acc[mi][nj][2], acc[mi][nj][3])};
          *(u32x2*)(st + (wave * 32 + 16 * mi + fr) * 240 + nj * 32 + fq * 8) = o;
        }
    }
    lds_barrier();
#pragma unroll
    for (int i = 0; i < 4; ++i) {
      const int c = t + 512 * i;
      if (c < 1792) {
        const int row = c / 14, ch = c - row * 14;
        u32x4 w = *(const u32x4*)(st + row * 240 + ch * 16);
        u32x4 o;
#pragma unroll
        for (int e = 0; e < 4; ++e) o[e] = pack2(gelu_fast(lo2f(w[e])), gelu_fast(hi2f(w[e])));
        *(u32x4*)(z1 + (size_t)(m0 + row) * FP + n0 + ch * 8) = o;
      }
    }
  } else {
    gemm_core<128, 112, G1_NS, 10, false, 1, 0>(xn + (size_t)m0 * D_, D_, w_inT + (size_t)n0 * D_, D_, lds, acc, [] {});
    const int nt2 = nt - 8;
    const int f0 = nt2 * 112;
    if (wave < 4) {
#pragma unroll
      for (int mi = 0; mi < 2; ++mi)
#pragma unroll
        for (int nj = 0; nj < 7; ++nj) {
          u32x2 o = {pack2(acc[mi][nj][0], acc[mi][nj][1]), pack2(acc[mi][nj][2], acc[mi][nj][3])};
          *(u32x2*)(st + (16 * nj + fr) * 272 + (wave * 32 + 16 * mi + 4 * fq) * 2) = o;
        }
    }
    lds_barrier();
    float sum[8], sq[8];
#pragma unroll
    for (int e = 0; e < 8; ++e) { sum[e] = 0.f; sq[e] = 0.f; }
    const int sc = t & 15;
    u32x4 zo[4];
#pragma unroll
    for (int i = 0; i < 4; ++i) {
      const int fl = (t >> 4) + 32 * i;
      zo[i] = (u32x4){0u, 0u, 0u, 0u};
      if (fl < 112) {
        u32x4 w = *(const u32x4*)(st + fl * 272 + sc * 16);
#pragma unroll
        for (int e = 0; e < 4; ++e) zo[i][e] = pack2(gelu_fast(lo2f(w[e])), gelu_fast(hi2f(w[e])));
        if (f0 + fl < FH) {
#pragma unroll
          for (int e = 0; e < 4; ++e) {
            float a = lo2f(zo[i][e]), bq = hi2f(zo[i][e]);
            sum[2 * e] += a; sq[2 * e] += a * a;
            sum[2 * e + 1] += bq; sq[2 * e + 1] += bq * bq;
          }
        }
      }
    }
#pragma unroll
    for (int e = 0; e < 8; ++e) {
      sum[e] += __shfl_xor(sum[e], 16); sum[e] += __shfl_xor(sum[e], 32);
      sq[e] += __shfl_xor(sq[e], 16); sq[e] += __shfl_xor(sq[e], 32);
    }
    float2* red = (float2*)(lds + (11 % G1_NS) * (240 * 128));
    float* sm = (float*)(red + 8 * 128);
    if (lane < 16) {
#pragma unroll
      for (int e = 0; e < 8; ++e) red[wave * 128 + sc * 8 + e] = make_float2(sum[e], sq[e]);
    }
    lds_barrier();
    if (t < 128) {
      float a = 0.f, q = 0.f;
#pragma unroll
      for (int w = 0; w < 8; ++w) { float2 v = red[w * 128 + t]; a += v.x; q += v.y; }
      u32 ab = __float_as_uint(a), qb = __float_as_uint(q);
      if (ab == 0xffffffffu) ab = 0x7fc00000u;
      if (qb == 0xffffffffu) qb = 0x7fc00000u;
      unsigned long long* pp = part + (size_t)mt * 1024 + t;
      __hip_atomic_store(pp + nt2 * 128, (unsigned long long)ab | ((unsigned long long)qb << 32), __ATOMIC_RELAXED,
                         __HIP_MEMORY_SCOPE_AGENT);
      unsigned long long pv[8];
      for (int spin = 0; spin < 400000; ++spin) {
        bool ok = true;
#pragma unroll
        for (int j = 0; j < 8; ++j) pv[j] = __hip_atomic_load(pp + j * 128, __ATOMIC_RELAXED, __HIP_MEMORY_SCOPE_AGENT);
#pragma unroll
        for (int j = 0; j < 8; ++j)
          ok = ok && ((u32)pv[j] != 0xffffffffu) && ((u32)(pv[j] >> 32) != 0xffffffffu);
        if (ok) break;
        __builtin_amdgcn_s_sleep(1);
      }
      float ts = 0.f, tq = 0.f;
#pragma unroll
      for (int j = 0; j < 8; ++j) { ts += __uint_as_float((u32)pv[j]); tq += __uint_as_float((u32)(pv[j] >> 32)); }
      float mean = ts * (1.0f / FH);
      float var = fmaxf(tq * (1.0f / FH) - mean * mean, 0.f);
      sm[t] = rbf(mean);
      sm[128 + t] = 1.0f / rbf(sqrtf(rbf(rbf(var) + 1e-5f)));
    }
    lds_barrier();
    {
      float mm[8], ii[8];
#pragma unroll
      for (int e = 0; e < 8; ++e) { mm[e] = sm[sc * 8 + e]; ii[e] = sm[128 + sc * 8 + e]; }
#pragma unroll
      for (int i = 0; i < 4; ++i) {
        const int fl = (t >> 4) + 32 * i;
        if (fl < 112) {
          u32x4 o;
#pragma unroll
          for (int e = 0; e < 4; ++e)
            o[e] = pack2(rbf(lo2f(zo[i][e]) - mm[2 * e]) * ii[2 * e], rbf(hi2f(zo[i][e]) - mm[2 * e + 1]) * ii[2 * e + 1]);
          *(u32x4*)(z2nT + (size_t)(f0 + fl) * S_ + m0 + sc * 8) = o;
        }
      }
    }
  }
}

constexpr int G2_NS = 5;
__global__ __launch_bounds__(512) void gemm2_kernel(const u16* __restrict__ w_sp, const u16* __restrict__ z2nT,
                                                    const u16* __restrict__ b_sp, const u16* __restrict__ z1,
                                                    u16* __restrict__ u) {
  __shared__ __attribute__((aligned(16))) char lds[G2_NS * 176 * 128];
  const int b = blockIdx.x, xcd = b & 7, idx = b >> 3;
  const int mt = (xcd >> 1) * 8 + (idx & 7);
  const int nt = (xcd & 1) * 4 + (idx >> 3);
  const int m0 = mt * 64, n0 = nt * 112;
  const int t = threadIdx.x, lane = t & 63, wave = t >> 6;
  const int fr = lane & 15, fq = lane >> 4;
  f32x4 acc[1][7];
#pragma unroll
  for (int j = 0; j < 7; ++j) acc[0][j] = (f32x4){0.f, 0.f, 0.f, 0.f};
  u32x4 zr[4];
  char* st = lds + (32 % G2_NS) * (176 * 128);
  float bias = 0.f;
  if (wave < 4) bias = bf2f(b_sp[m0 + wave * 16 + fr]);
  gemm_core<64, 112, G2_NS, 32, true, 2, 4>(w_sp + (size_t)m0 * S_, S_, z2nT + (size_t)n0 * S_, S_, lds, acc, [&] {
#pragma unroll
    for (int i = 0; i < 4; ++i) {
      int c = (t - 256) + 256 * i; if (c > 895) c = 895;
      const int row = c / 14, ch = c - row * 14;
      zr[i] = *(const u32x4*)(z1 + (size_t)(m0 + row) * FP + n0 + ch * 8);
    }
  });
  if (wave < 4) {
#pragma unroll
    for (int nj = 0; nj < 7; ++nj) {
      float g0 = rbf(acc[0][nj][0]) + bias, g1 = rbf(acc[0][nj][1]) + bias;
      float g2 = rbf(acc[0][nj][2]) + bias, g3 = rbf(acc[0][nj][3]) + bias;
      u32x2 o = {pack2(g0, g1), pack2(g2, g3)};
      *(u32x2*)(st + (wave * 16 + fr) * 240 + nj * 32 + fq * 8) = o;
    }
    lds_barrier();
  } else {
    lds_barrier();
#pragma unroll
    for (int i = 0; i < 4; ++i) {
      const int c = (t - 256) + 256 * i;
      if (c < 896) {
        const int row = c / 14, ch = c - row * 14;
        u32x4 g = *(const u32x4*)(st + row * 240 + ch * 16);
        u32x4 o;
#pragma unroll
        for (int e = 0; e < 4; ++e) o[e] = pack2(lo2f(zr[i][e]) * lo2f(g[e]), hi2f(zr[i][e]) * hi2f(g[e]));
        *(u32x4*)(u + (size_t)(m0 + row) * FP + n0 + ch * 8) = o;
      }
    }
  }
}

constexpr int G3_NS = 6;
__global__ __launch_bounds__(512) void gemm3_kernel(const u16* __restrict__ u, const u16* __restrict__ w_outT,
                                                    const u16* __restrict__ xres, u16* __restrict__ out) {
  __shared__ __attribute__((aligned(16))) char lds[G3_NS * 144 * 128];
  const int b = blockIdx.x, xcd = b & 7, idx = b >> 3;
  const int mt = (xcd >> 1) * 8 + (idx & 7);
  const int nt = (xcd & 1) * 4 + (idx >> 3);
  const int m0 = mt * 64, n0 = nt * 80;
  const int t = threadIdx.x, lane = t & 63, wave = t >> 6;
  const int fr = lane & 15, fq = lane >> 4;
  f32x4 acc[1][5];
#pragma unroll
  for (int j = 0; j < 5; ++j) acc[0][j] = (f32x4){0.f, 0.f, 0.f, 0.f};
  u32x4 xr[3];
  char* st = lds + (14 % G3_NS) * (144 * 128);
  gemm_core<64, 80, G3_NS, 14, true, 3, 3>(u + (size_t)m0 * FP, FP, w_outT + (size_t)n0 * FP, FP, lds, acc, [&] {
#pragma unroll
    for (int i = 0; i < 3; ++i) {
      int c = (t - 256) + 256 * i; if (c > 639) c = 639;
      const int row = c / 10, ch = c - row * 10;
      xr[i] = *(const u32x4*)(xres + (size_t)(m0 + row) * D_ + n0 + ch * 8);
    }
  });
  if (wave < 4) {
#pragma unroll
    for (int nj = 0; nj < 5; ++nj) {
      u32x2 o = {pack2(acc[0][nj][0], acc[0][nj][1]), pack2(acc[0][nj][2], acc[0][nj][3])};
      *(u32x2*)(st + (wave * 16 + fr) * 176 + nj * 32 + fq * 8) = o;
    }
    lds_barrier();
  } else {
    lds_barrier();
#pragma unroll
    for (int i = 0; i < 3; ++i) {
      const int c = (t - 256) + 256 * i;
      if (c < 640) {
        const int row = c / 10, ch = c - row * 10;
        u32x4 g = *(const u32x4*)(st + row * 176 + ch * 16);
        u32x4 o;
#pragma unroll
        for (int e = 0; e < 4; ++e) o[e] = pack2(lo2f(xr[i][e]) + lo2f(g[e]), hi2f(xr[i][e]) + hi2f(g[e]));
        *(u32x4*)(out + (size_t)(m0 + row) * D_ + n0 + ch * 8) = o;
      }
    }
  }
}

extern "C" void kernel_launch(void* const* d_in, const int* in_sizes, int n_in, void* d_out, int out_size,
                              void* d_ws, size_t ws_size, hipStream_t stream) {
  const u16* A_q = (const u16*)d_in[0];
  const u16* A_k = (const u16*)d_in[1];
  const u16* A_v = (const u16*)d_in[2];
  const u16* A_shift_k = (const u16*)d_in[3];
  const u16* A_ssm_k = (const u16*)d_in[4];
  const u16* A_ssm_D = (const u16*)d_in[5];
  const u16* B_w_in = (const u16*)d_in[7];
  const u16* B_w_sp = (const u16*)d_in[8];
  const u16* B_b_sp = (const u16*)d_in[9];
  const u16* B_w_out = (const u16*)d_in[10];
  u16* out = (u16*)d_out;

  char* ws = (char*)d_ws;
  size_t off = 0;
  auto take = [&](size_t bytes) { char* p = ws + off; off += (bytes + 255) & ~(size_t)255; return p; };
  u16* kvT = (u16*)take((size_t)D_ * S_ * 2);
  u16* YT = (u16*)take((size_t)D_ * S_ * 2);
  u16* w_inT = (u16*)take((size_t)2 * FP * D_ * 2);
  u16* w_outT = (u16*)take((size_t)D_ * FP * 2);
  u16* xa = (u16*)take((size_t)S_ * D_ * 2);
  u16* xb = (u16*)take((size_t)S_ * D_ * 2);
  u16* xn = (u16*)take((size_t)S_ * D_ * 2);
  u16* z1 = (u16*)take((size_t)S_ * FP * 2);
  u16* z2nT = (u16*)take((size_t)FP * S_ * 2);
  u16* u = (u16*)take((size_t)S_ * FP * 2);
  unsigned long long* part = (unsigned long long*)take((size_t)2 * 16 * 1024 * 8);

  pre_kernel<<<PRE_KV_BLOCKS + AUX_BLOCKS + PRE_TOUCH_BLOCKS, 256, 0, stream>>>(A_k, A_v, A_shift_k, A_ssm_k, B_w_in,
                                                                                 B_w_out, kvT, w_inT, w_outT,
                                                                                 (u32x4*)part);
  conv_kernel<<<D_, 256, 0, stream>>>(kvT, A_ssm_k, A_ssm_D, YT, B_w_sp, A_q);
  rowop_kernel<<<256, 512, 0, stream>>>(A_q, YT, xa, xn);
  gemm1_kernel<<<256, 512, 0, stream>>>(xn, w_inT, z1, z2nT, part);
  gemm2_kernel<<<256, 512, 0, stream>>>(B_w_sp, z2nT, B_b_sp, z1, u);
  gemm3_kernel<<<256, 512, 0, stream>>>(u, w_outT, xa, xb);
  rowop_kernel<<<256, 512, 0, stream>>>(xb, YT, xa, xn);
  gemm1_kernel<<<256, 512, 0, stream>>>(xn, w_inT, z1, z2nT, part + 16 * 1024);
  gemm2_kernel<<<256, 512, 0, stream>>>(B_w_sp, z2nT, B_b_sp, z1, u);
  gemm3_kernel<<<256, 512, 0, stream>>>(u, w_outT, xa, out);
}
```

```cpp
#include <hip/hip_runtime.h>
#include <hip/hip_bf16.h>
#include <stdint.h>

typedef unsigned short u16;
typedef unsigned int u32;
typedef __attribute__((ext_vector_type(8))) short bf16x8;
typedef __attribute__((ext_vector_type(4))) float f32x4;
typedef __attribute__((ext_vector_type(4))) unsigned int u32x4;
typedef __attribute__((ext_vector_type(2))) unsigned int u32x2;

#define DEV __device__ __forceinline__

constexpr int S_ = 2048, D_ = 640, FH = 864, FP = 896;

DEV float bf2f(u16 v) { return __uint_as_float(((u32)v) << 16); }
DEV float lo2f(u32 v) { return __uint_as_float(v << 16); }
DEV float hi2f(u32 v) { return __uint_as_float(v & 0xffff0000u); }
DEV float rbf(float f) { return (float)(__bf16)f; }
DEV u16 f2bf(float f) { __bf16 b = (__bf16)f; return __builtin_bit_cast(u16, b); }
DEV u32 pack2(float a, float b) { return (u32)f2bf(a) | ((u32)f2bf(b) << 16); }

DEV float wave_sum(float v) {
#pragma unroll
  for (int o = 32; o > 0; o >>= 1) v += __shfl_xor(v, o);
  return v;
}

DEV float gelu_tanh(float x) {
  float y = 0.7978845608028654f * (x + 0.044715f * x * x * x);
  float e = __expf(-2.0f * y);
  return __fdividef(x, 1.0f + e);
}

constexpr int PRE_KV_BLOCKS = 320, PRE_TOUCH_BLOCKS = 64;
constexpr int AUX_WIN_BLOCKS = 270, AUX_WOUT_BLOCKS = 140, AUX_PAD_BLOCKS = 4;
constexpr int AUX_BLOCKS = AUX_WIN_BLOCKS + AUX_WOUT_BLOCKS + AUX_PAD_BLOCKS;

DEV void touch(const void* p, int n16, int blk, int nblk) {
  const u32x4* q = (const u32x4*)p;
  const int per = (n16 + nblk - 1) / nblk;
  const int lo = blk * per, hi = min(lo + per, n16);
  for (int i = lo + (int)threadIdx.x; i < hi; i += 256 * 8) {
    u32x4 w[8];
#pragma unroll
    for (int j = 0; j < 8; ++j) { int c = i + 256 * j; w[j] = q[c < hi ? c : i]; }
#pragma unroll
    for (int j = 0; j < 8; ++j) asm volatile("" ::"v"(w[j]));
  }
}

DEV void aux_work(int b, u16 (*tile)[72], const u16* __restrict__ w_in, const u16* __restrict__ w_out,
                  u16* __restrict__ w_inT, u16* __restrict__ w_outT) {
  const int t = threadIdx.x;
  if (b < AUX_WIN_BLOCKS + AUX_WOUT_BLOCKS) {
    const u16* src; u16* dst; int ld_src, ld_dst, r0, c0, rmax, drow0;
    if (b < AUX_WIN_BLOCKS) {
      int kt = b % 10, nt = b / 10;
      src = w_in; ld_src = 2 * FH; r0 = kt * 64; c0 = nt * 64; rmax = D_;
      dst = w_inT; ld_dst = D_; drow0 = 0;
    } else {
      int bb = b - AUX_WIN_BLOCKS;
      int kt = bb % 14, dt = bb / 14;
      src = w_out; ld_src = D_; r0 = kt * 64; c0 = dt * 64; rmax = FH;
      dst = w_outT; ld_dst = FP; drow0 = 1;
    }
#pragma unroll
    for (int h = 0; h < 2; ++h) {
      int idx = t + 256 * h;
      int r = idx >> 3, ch = idx & 7;
      u32x4 w = (u32x4){0u, 0u, 0u, 0u};
      if (r0 + r < rmax) w = *(const u32x4*)(src + (size_t)(r0 + r) * ld_src + c0 + ch * 8);
#pragma unroll
      for (int e = 0; e < 4; ++e) {
        tile[ch * 8 + 2 * e][r] = (u16)(w[e] & 0xffffu);
        tile[ch * 8 + 2 * e + 1][r] = (u16)(w[e] >> 16);
      }
    }
    __syncthreads();
#pragma unroll
    for (int h = 0; h < 2; ++h) {
      int idx = t + 256 * h;
      int cr = idx >> 3, ch = idx & 7;
      int n = c0 + cr;
      int drow = n;
      if (!drow0) {
        const int half = n >= FH ? 1 : 0, c = n - half * FH;
        drow = 112 * (c / 56) + 56 * half + (c % 56);
      }
      u32x4 o = *(const u32x4*)(&tile[cr][ch * 8]);
      *(u32x4*)(dst + (size_t)drow * ld_dst + r0 + ch * 8) = o;
    }
    return;
  }
  b -= AUX_WIN_BLOCKS + AUX_WOUT_BLOCKS;
  if (b < AUX_PAD_BLOCKS) {
    for (int i = b * 256 + t; i < 5120; i += AUX_PAD_BLOCKS * 256) {
      int row = i / 80, ch = i % 80;
      int rr = 112 * 15 + 24 + (row < 32 ? row : row + 24);
      *(u32x4*)(w_inT + (size_t)rr * D_ + ch * 8) = (u32x4){0u, 0u, 0u, 0u};
    }
    return;
  }
}

__global__ __launch_bounds__(256) void pre_kernel(const u16* __restrict__ k, const u16* __restrict__ v,
                                                  const u16* __restrict__ shift_k, const u16* __restrict__ ssm_k,
                                                  const u16* __restrict__ w_in, const u16* __restrict__ w_out,
                                                  u16* __restrict__ kvT, u16* __restrict__ w_inT,
                                                  u16* __restrict__ w_outT, u32x4* __restrict__ part) {
  __shared__ u16 tile[64][72];
  const int t = threadIdx.x;
  int b = blockIdx.x;
  if (b < PRE_KV_BLOCKS) {
    const int s0 = (b & 31) * 64, d0 = (b >> 5) * 64;
    const int c8 = t & 7, r0 = t >> 3;
    const int d = d0 + c8 * 8;
    float sk[8][4];
#pragma unroll
    for (int j = 0; j < 4; ++j) {
      u32x4 w = *(const u32x4*)(shift_k + (d + 2 * j) * 4);
      sk[2 * j][0] = lo2f(w[0]); sk[2 * j][1] = hi2f(w[0]); sk[2 * j][2] = lo2f(w[1]); sk[2 * j][3] = hi2f(w[1]);
      sk[2 * j + 1][0] = lo2f(w[2]); sk[2 * j + 1][1] = hi2f(w[2]); sk[2 * j + 1][2] = lo2f(w[3]); sk[2 * j + 1][3] = hi2f(w[3]);
    }
#pragma unroll
    for (int h = 0; h < 2; ++h) {
      const int r = r0 + 32 * h;
      const int s = s0 + r;
      u32x4 kk[4];
#pragma unroll
      for (int j = 0; j < 4; ++j) {
        if (s - j >= 0) kk[j] = *(const u32x4*)(k + (size_t)(s - j) * D_ + d);
        else kk[j] = (u32x4){0u, 0u, 0u, 0u};
      }
      u32x4 vv = *(const u32x4*)(v + (size_t)s * D_ + d);
#pragma unroll
      for (int e = 0; e < 8; ++e) {
        float acc = 0.f;
#pragma unroll
        for (int tt = 0; tt < 4; ++tt) {
          u32 w = kk[3 - tt][e >> 1];
          float kf = (e & 1) ? hi2f(w) : lo2f(w);
          float p = rbf(kf * sk[e][3 - tt]);
          acc = rbf(acc + p);
        }
        u32 w = vv[e >> 1];
        float vf = (e & 1) ? hi2f(w) : lo2f(w);
        tile[c8 * 8 + e][r] = f2bf(acc * vf);
      }
    }
    __syncthreads();
#pragma unroll
    for (int h = 0; h < 2; ++h) {
      int idx = t + 256 * h;
      int dr = idx >> 3, ch = idx & 7;
      u32x4 o = *(const u32x4*)(&tile[dr][ch * 8]);
      *(u32x4*)(kvT + (size_t)(d0 + dr) * S_ + s0 + ch * 8) = o;
    }
    return;
  }
  b -= PRE_KV_BLOCKS;
  if (b < AUX_BLOCKS) { aux_work(b, tile, w_in, w_out, w_inT, w_outT); return; }
  b -= AUX_BLOCKS;
  part[b * 512 + t] = (u32x4){0xffffffffu, 0xffffffffu, 0xffffffffu, 0xffffffffu};
  part[b * 512 + 256 + t] = (u32x4){0xffffffffu, 0xffffffffu, 0xffffffffu, 0xffffffffu};
  touch(ssm_k, D_ * S_ / 8, b, PRE_TOUCH_BLOCKS);
}

constexpr int CV_E_BYTES = 2448 * 16;
constexpr int CV_X_BYTES = 32 * 272;

__global__ __launch_bounds__(256) void conv_kernel(const u16* __restrict__ kvT, const u16* __restrict__ ssm_k,
                                                   const u16* __restrict__ ssm_D, u16* __restrict__ YT,
                                                   const u16* __restrict__ w_sp, const u16* __restrict__ q) {
  __shared__ __attribute__((aligned(16))) char smem[CV_E_BYTES + CV_X_BYTES];
  char* Et = smem;
  char* Xp = smem + CV_E_BYTES;
  const int t = threadIdx.x, lane = t & 63;
  const int wave = __builtin_amdgcn_readfirstlane(t >> 6);
  const int d = blockIdx.x;
  const u16* h = ssm_k + (size_t)d * S_;
  const u16* x = kvT + (size_t)d * S_;
  {
    u32x4 w1 = *(const u32x4*)(h + 8 * t);
    u32x4 w0 = (u32x4){0u, 0u, 0u, 0u};
    if (t > 0) w0 = *(const u32x4*)(h + 8 * t - 8);
    u32 w[8] = {w0[0], w0[1], w0[2], w0[3], w1[0], w1[1], w1[2], w1[3]};
    char* base = Et + (144 + 9 * t) * 16;
#pragma unroll
    for (int r = 0; r < 8; ++r) {
      u32x4 g;
#pragma unroll
      for (int dd = 0; dd < 4; ++dd) {
        if ((r & 1) == 0) {
          int p = (8 + r - 2 * dd) >> 1;
          g[dd] = (w[p] & 0xffffu) | (w[p - 1] & 0xffff0000u);
        } else {
          int p = (8 + r - 2 * dd - 1) >> 1;
          g[dd] = (w[p] >> 16) | (w[p] << 16);
        }
      }
      *(u32x4*)(base + r * 16) = g;
    }
    if (t < 144) *(u32x4*)(Et + t * 16) = (u32x4){0u, 0u, 0u, 0u};
    u32x4 xv = *(const u32x4*)(x + 8 * t);
    *(u32x4*)(Xp + (16 + (t >> 4)) * 272 + (t & 15) * 16) = xv;
    for (int i = t; i < 16 * 17; i += 256) *(u32x4*)(Xp + i * 16) = (u32x4){0u, 0u, 0u, 0u};
  }
  __syncthreads();
  u32x4 pf[5];
  {
    const int base = d * 256 + t;
#pragma unroll
    for (int j = 0; j < 4; ++j) {
      int c = base + j * (D_ * 256); if (c >= S_ * S_ / 8) c = base;
      pf[j] = ((const u32x4*)w_sp)[c];
    }
    pf[4] = ((const u32x4*)q)[base];
  }
  const int p = wave & 1, hk = wave >> 1;
  const int m = lane & 15, kg = lane >> 4;
  const char* abase = Et + (m + (m >> 3) - 9 * kg) * 16 + 288 * p + 2304;
  const char* bbase = Xp + (m + 16) * 272 + kg * 16;
  f32x4 acc[4];
#pragma unroll
  for (int i = 0; i < 4; ++i) acc[i] = (f32x4){0.f, 0.f, 0.f, 0.f};
  bf16x8 F[4];
  const int D0 = 8 * hk;
  F[1] = *(const bf16x8*)(abase + 576 * (4 * D0 - 3));
  F[2] = *(const bf16x8*)(abase + 576 * (4 * D0 - 2));
  F[3] = *(const bf16x8*)(abase + 576 * (4 * D0 - 1));
  for (int dl = 0; dl < 8; ++dl) {
    const int Dl = D0 + dl;
    const char* ab = abase + 576 * 4 * Dl;
    const char* bb = bbase - 272 * Dl;
#pragma unroll
    for (int qq = 0; qq < 4; ++qq) {
      const int q = 3 - qq;
      F[qq] = *(const bf16x8*)(ab + 576 * qq);
      bf16x8 B = *(const bf16x8*)(bb + 64 * q);
#pragma unroll
      for (int i = 0; i < 4; ++i) {
        acc[i] = __builtin_amdgcn_mfma_f32_16x16x32_bf16(F[(i - q) & 3], B, acc[i], 0, 0, 0);
      }
    }
  }
  __syncthreads();
  float* yp = (float*)Et;
  u16* orow = (u16*)(Et + 8704);
  const int a = m, g = kg;
  if (hk == 1) {
#pragma unroll
    for (int i = 0; i < 4; ++i) {
      int mi = p + 2 * i;
      *(f32x4*)(yp + a * 132 + 16 * mi + 4 * g) = acc[i];
    }
  }
  __syncthreads();
  if (hk == 0) {
    const float Dv = bf2f(ssm_D[d]);
#pragma unroll
    for (int i = 0; i < 4; ++i) {
      int mi = p + 2 * i;
      f32x4 o = *(const f32x4*)(yp + a * 132 + 16 * mi + 4 * g);
      u32x2 xv = *(const u32x2*)(Xp + (a + 16) * 272 + (16 * mi + 4 * g) * 2);
      float xs[4] = {lo2f(xv[0]), hi2f(xv[0]), lo2f(xv[1]), hi2f(xv[1])};
      float res[4];
#pragma unroll
      for (int r = 0; r < 4; ++r) {
        float y = rbf(acc[i][r] + o[r]);
        float kd = rbf(xs[r] * Dv);
        res[r] = y + kd;
      }
      u32x2 ov = {pack2(res[0], res[1]), pack2(res[2], res[3])};
      *(u32x2*)(orow + 128 * a + 16 * mi + 4 * g) = ov;
    }
  }
  __syncthreads();
  *(u32x4*)(YT + (size_t)d * S_ + 8 * t) = *(const u32x4*)(orow + 8 * t);
#pragma unroll
  for (int j = 0; j < 5; ++j) asm volatile("" ::"v"(pf[j]));
}

DEV void ln10(const float (&v)[10], float (&o)[10]) {
  float s = 0.f;
#pragma unroll
  for (int i = 0; i < 10; ++i) s += v[i];
  s = wave_sum(s);
  float mean = s * (1.0f / 640.0f);
  float q = 0.f;
#pragma unroll
  for (int i = 0; i < 10; ++i) { float dd = v[i] - mean; q += dd * dd; }
  q = wave_sum(q);
  float var = rbf(q * (1.0f / 640.0f));
  float mb = rbf(mean);
  float den = rbf(sqrtf(rbf(var + 1e-5f)));
  float inv = 1.0f / den;
#pragma unroll
  for (int i = 0; i < 10; ++i) o[i] = rbf(rbf(v[i] - mb) * inv);
}

__global__ __launch_bounds__(512) void rowop_kernel(const u16* __restrict__ xin, const u16* __restrict__ YT,
                                                    u16* __restrict__ xo, u16* __restrict__ xn) {
  __shared__ u16 yt[8][648];
  const int t = threadIdx.x, lane = t & 63, r = t >> 6;
  const int s0 = ((blockIdx.x & 7) * 32 + (blockIdx.x >> 3)) * 8;
  for (int d = t; d < D_; d += 512) {
    u32x4 w = *(const u32x4*)(YT + (size_t)d * S_ + s0);
#pragma unroll
    for (int e = 0; e < 4; ++e) {
      yt[2 * e][d] = (u16)(w[e] & 0xffffu);
      yt[2 * e + 1][d] = (u16)(w[e] >> 16);
    }
  }
  float v[10];
  const u16* xr = xin + (size_t)(s0 + r) * D_;
#pragma unroll
  for (int i = 0; i < 5; ++i) {
    u32 w = *(const u32*)(xr + 2 * lane + 128 * i);
    v[2 * i] = lo2f(w); v[2 * i + 1] = hi2f(w);
  }
  float a[10];
  ln10(v, a);
  __syncthreads();
  float x1[10];
#pragma unroll
  for (int i = 0; i < 5; ++i) {
    u32 w = *(const u32*)(&yt[r][2 * lane + 128 * i]);
    x1[2 * i] = rbf(v[2 * i] + rbf(a[2 * i] * lo2f(w)));
    x1[2 * i + 1] = rbf(v[2 * i + 1] + rbf(a[2 * i + 1] * hi2f(w)));
  }
  float n1[10];
  ln10(x1, n1);
  u16* xor_ = xo + (size_t)(s0 + r) * D_;
  u16* xnr = xn + (size_t)(s0 + r) * D_;
#pragma unroll
  for (int i = 0; i < 5; ++i) {
    *(u32*)(xor_ + 2 * lane + 128 * i) = pack2(x1[2 * i], x1[2 * i + 1]);
    *(u32*)(xnr + 2 * lane + 128 * i) = pack2(n1[2 * i], n1[2 * i + 1]);
  }
}

template <int N> DEV void wait_vm() {
  asm volatile("s_waitcnt vmcnt(%0)" ::"n"(N) : "memory");
}

template <int BM, int BN, int NS, int NK, bool MIXED, int KID, int EXTRA, class PRE>
DEV void gemm_core(const u16* __restrict__ A, int lda, const u16* __restrict__ Bt, int ldb, char* lds,
                   f32x4 (&acc)[BM / 64][BN / 16 + (MIXED ? 1 : 0)], PRE&& pre) {
  constexpr int T = (BM + BN) / 8;
  constexpr int NI = (T + 3) / 4;
  constexpr int REM = T % 4;
  constexpr int STAGE = (BM + BN) * 128;
  constexpr int MI = BM / 64, NJ = BN / 16;
  const int t = threadIdx.x, lane = t & 63;
  const int wave = __builtin_amdgcn_readfirstlane(t >> 6);
  if (wave >= 4) {
    const int lw = wave - 4;
    const u16* src[NI];
#pragma unroll
    for (int j = 0; j < NI; ++j) {
      int i = lw + 4 * j;
      if (i >= T) i = T - 1;
      int row = 8 * i + (lane >> 3);
      int cg = (lane & 7) ^ (row & 7);
      src[j] = (8 * i < BM) ? (A + (size_t)row * lda + cg * 8) : (Bt + (size_t)(row - BM) * ldb + cg * 8);
    }
    const bool full = (REM == 0) || (lw < REM);
    auto issue = [&](int kt) {
      char* dst = lds + (kt % NS) * STAGE + lw * 1024;
#pragma unroll
      for (int j = 0; j < NI; ++j) {
        if (j < NI - 1 || full)
          __builtin_amdgcn_global_load_lds((const u32*)(src[j] + kt * 64), (u32*)(dst + j * 4096), 16, 0, 0);
      }
    };
    auto waitfor = [&](int rem) {
      if (rem <= 0) wait_vm<0>();
      else if (rem == 1) { if (full) wait_vm<NI>(); else wait_vm<NI - 1>(); }
      else if (rem == 2) { if (full) wait_vm<2 * NI>(); else wait_vm<2 * (NI - 1)>(); }
      else if (rem == 3) { if (full) wait_vm<3 * NI>(); else wait_vm<3 * (NI - 1)>(); }
      else if (rem == 4) { if (full) wait_vm<4 * NI>(); else wait_vm<4 * (NI - 1)>(); }
      else if (rem == 5) { if (full) wait_vm<5 * NI>(); else wait_vm<5 * (NI - 1)>(); }
      else wait_vm<0>();
    };
    auto waitforx = [&](int rem) {
      if (rem <= 0) wait_vm<EXTRA>();
      else if (rem == 1) { if (full) wait_vm<EXTRA + NI>(); else wait_vm<EXTRA + NI - 1>(); }
      else if (rem == 2) { if (full) wait_vm<EXTRA + 2 * NI>(); else wait_vm<EXTRA + 2 * (NI - 1)>(); }
      else if (rem == 3) { if (full) wait_vm<EXTRA + 3 * NI>(); else wait_vm<EXTRA + 3 * (NI - 1)>(); }
      else if (rem == 4) { if (full) wait_vm<EXTRA + 4 * NI>(); else wait_vm<EXTRA + 4 * (NI - 1)>(); }
      else if (rem == 5) { if (full) wait_vm<EXTRA + 5 * NI>(); else wait_vm<EXTRA + 5 * (NI - 1)>(); }
      else wait_vm<0>();
    };
    static_assert(NS - 2 <= 5, "");
#pragma unroll
    for (int s = 0; s < NS - 1; ++s) issue(s);
    constexpr int MAINK = NK - (NS - 1);
    for (int kt = 0; kt < MAINK; ++kt) {
      waitfor(NS - 2);
      __builtin_amdgcn_s_barrier();
      issue(kt + NS - 1);
    }
    pre();
#pragma unroll
    for (int kt = MAINK; kt < NK; ++kt) {
      waitforx(NK - 1 - kt);
      __builtin_amdgcn_s_barrier();
    }
    return;
  }
  const int fr = lane & 15, fq = lane >> 4;
  int aoff[MI], boff[NJ];
#pragma unroll
  for (int mi = 0; mi < MI; ++mi) aoff[mi] = (wave * (BM / 4) + 16 * mi + fr) * 128;
#pragma unroll
  for (int nj = 0; nj < NJ; ++nj) boff[nj] = (BM + 16 * nj + fr) * 128;
  const int sw = fr & 7;
  bf16x8 fa[2][2][MI], fb[2][2][NJ];
  auto rd = [&](int kt, bf16x8 (&a)[2][MI], bf16x8 (&bb)[2][NJ]) {
    const char* base = lds + (kt % NS) * STAGE;
#pragma unroll
    for (int kk = 0; kk < 2; ++kk) {
      const int ch = ((4 * kk + fq) ^ sw) * 16;
#pragma unroll
      for (int mi = 0; mi < MI; ++mi) a[kk][mi] = *(const bf16x8*)(base + aoff[mi] + ch);
#pragma unroll
      for (int nj = 0; nj < NJ; ++nj) bb[kk][nj] = *(const bf16x8*)(base + boff[nj] + ch);
    }
  };
  auto mma = [&](bf16x8 (&a)[2][MI], bf16x8 (&bb)[2][NJ]) {
#pragma unroll
    for (int kk = 0; kk < 2; ++kk)
#pragma unroll
      for (int mi = 0; mi < MI; ++mi)
#pragma unroll
        for (int nj = 0; nj < NJ; ++nj) {
          if (!MIXED || nj <= NJ / 2)
            acc[mi][nj] = __builtin_amdgcn_mfma_f32_16x16x32_bf16(bb[kk][nj], a[kk][mi], acc[mi][nj], 0, 0, 0);
          if (MIXED && nj >= NJ / 2)
            acc[mi][nj + 1] = __builtin_amdgcn_mfma_f32_16x16x32_bf16(a[kk][mi], bb[kk][nj], acc[mi][nj + 1], 0, 0, 0);
        }
  };
  static_assert(NK % 2 == 0, "");
  auto ilv = [&]() {
    constexpr int NR = 2 * (MI + NJ), NM = 2 * MI * (NJ + (MIXED ? 1 : 0));
    if constexpr (NM >= NR) {
      constexpr int PER = NM / NR;
#pragma unroll
      for (int i = 0; i < NR; ++i) {
        __builtin_amdgcn_sched_group_barrier(0x100, 1, 0);
        __builtin_amdgcn_sched_group_barrier(0x008, PER, 0);
      }
      __builtin_amdgcn_sched_group_barrier(0x008, NM - PER * NR, 0);
    } else {
#pragma unroll
      for (int i = 0; i < NM; ++i) {
        __builtin_amdgcn_sched_group_barrier(0x100, 1, 0);
        __builtin_amdgcn_sched_group_barrier(0x008, 1, 0);
      }
      __builtin_amdgcn_sched_group_barrier(0x100, NR - NM, 0);
    }
  };
  auto lgk0 = [&]() { __builtin_amdgcn_sched_barrier(0); asm volatile("s_waitcnt lgkmcnt(0)" ::: "memory"); };
  __builtin_amdgcn_s_barrier();
  rd(0, fa[0], fb[0]);
  __builtin_amdgcn_sched_barrier(0);
  lgk0();
  __builtin_amdgcn_s_barrier();
  rd(1, fa[1], fb[1]);
  __builtin_amdgcn_sched_barrier(0);
  mma(fa[0], fb[0]);
  for (int kt = 2; kt < NK; kt += 2) {
    lgk0();
    __builtin_amdgcn_s_barrier();
    rd(kt, fa[0], fb[0]);
    mma(fa[1], fb[1]);
    ilv();
    lgk0();
    __builtin_amdgcn_s_barrier();
    rd(kt + 1, fa[1], fb[1]);
    mma(fa[0], fb[0]);
    ilv();
  }
  mma(fa[1], fb[1]);
}

DEV float gelu_fast(float x) {
  const float c1 = -2.0f * 0.7978845608028654f * 1.4426950408889634f;
  const float c2 = c1 * 0.044715f;
  float e = __builtin_amdgcn_exp2f(x * __builtin_fmaf(x * x, c2, c1));
  return x * __builtin_amdgcn_rcpf(1.0f + e);
}
DEV void lds_barrier() { asm volatile("s_waitcnt lgkmcnt(0)" ::: "memory"); __builtin_amdgcn_s_barrier(); asm volatile("" ::: "memory"); }

constexpr int G1_NS = 4;
__global__ __launch_bounds__(512) void gemm1_kernel(const u16* __restrict__ xn, const u16* __restrict__ w_inT,
                                                    u16* __restrict__ z1, u16* __restrict__ z2nT,
                                                    unsigned long long* __restrict__ part) {
  __shared__ __attribute__((aligned(16))) char lds[G1_NS * 240 * 128];
  const int b = blockIdx.x, xcd = b & 7, idx = b >> 3;
  const int mt = (xcd >> 1) * 4 + (idx & 3);
  const int nt = (xcd & 1) * 8 + (idx >> 2);
  const int m0 = mt * 128, c0 = nt * 56;
  const int t = threadIdx.x, lane = t & 63, wave = t >> 6;
  const int fr = lane & 15, fq = lane >> 4;
  f32x4 acc[2][8];
#pragma unroll
  for (int i = 0; i < 2; ++i)
#pragma unroll
    for (int j = 0; j < 8; ++j) acc[i][j] = (f32x4){0.f, 0.f, 0.f, 0.f};
  gemm_core<128, 112, G1_NS, 10, true, 1, 0>(xn + (size_t)m0 * D_, D_, w_inT + (size_t)nt * 112 * D_, D_, lds, acc, [] {});
  char* st1 = lds + 2 * (240 * 128);
  char* st2 = lds + 3 * (240 * 128);
  float2* red = (float2*)lds;
  float* sm = (float*)(red + 8 * 128);
  if (wave < 4) {
#pragma unroll
    for (int mi = 0; mi < 2; ++mi) {
#pragma unroll
      for (int nj = 0; nj < 4; ++nj) {
        u32x2 o = {pack2(acc[mi][nj][0], acc[mi][nj][1]), pack2(acc[mi][nj][2], acc[mi][nj][3])};
        if (nj < 3 || fq < 2) *(u32x2*)(st1 + (wave * 32 + 16 * mi + fr) * 144 + nj * 32 + fq * 8) = o;
      }
#pragma unroll
      for (int nj = 3; nj < 7; ++nj) {
        u32x2 o = {pack2(acc[mi][nj + 1][0], acc[mi][nj + 1][1]), pack2(acc[mi][nj + 1][2], acc[mi][nj + 1][3])};
        if (nj > 3 || fr >= 8) *(u32x2*)(st2 + (16 * nj + fr - 56) * 272 + (wave * 32 + 16 * mi + 4 * fq) * 2) = o;
      }
    }
  }
  lds_barrier();
  const int sc = t & 15;
  u32x4 zo[2];
  {
    float sum[8], sq[8];
#pragma unroll
    for (int e = 0; e < 8; ++e) { sum[e] = 0.f; sq[e] = 0.f; }
#pragma unroll
    for (int i = 0; i < 2; ++i) {
      const int fl = (t >> 4) + 32 * i;
      zo[i] = (u32x4){0u, 0u, 0u, 0u};
      if (fl < 56) {
        u32x4 w = *(const u32x4*)(st2 + fl * 272 + sc * 16);
#pragma unroll
        for (int e = 0; e < 4; ++e) zo[i][e] = pack2(gelu_fast(lo2f(w[e])), gelu_fast(hi2f(w[e])));
        if (c0 + fl < FH) {
#pragma unroll
          for (int e = 0; e < 4; ++e) {
            float a = lo2f(zo[i][e]), bq = hi2f(zo[i][e]);
            sum[2 * e] += a; sq[2 * e] += a * a;
            sum[2 * e + 1] += bq; sq[2 * e + 1] += bq * bq;
          }
        }
      }
    }
#pragma unroll
    for (int e = 0; e < 8; ++e) {
      sum[e] += __shfl_xor(sum[e], 16); sum[e] += __shfl_xor(sum[e], 32);
      sq[e] += __shfl_xor(sq[e], 16); sq[e] += __shfl_xor(sq[e], 32);
    }
    if (lane < 16) {
#pragma unroll
      for (int e = 0; e < 8; ++e) red[wave * 128 + sc * 8 + e] = make_float2(sum[e], sq[e]);
    }
  }
  lds_barrier();
  unsigned long long* pp = part + (size_t)mt * 2048 + (t & 127);
  if (t < 128) {
    float a = 0.f, q = 0.f;
#pragma unroll
    for (int w = 0; w < 8; ++w) { float2 v = red[w * 128 + t]; a += v.x; q += v.y; }
    u32 ab = __float_as_uint(a), qb = __float_as_uint(q);
    if (ab == 0xffffffffu) ab = 0x7fc00000u;
    if (qb == 0xffffffffu) qb = 0x7fc00000u;
    __hip_atomic_store(pp + nt * 128, (unsigned long long)ab | ((unsigned long long)qb << 32), __ATOMIC_RELAXED,
                       __HIP_MEMORY_SCOPE_AGENT);
  }
#pragma unroll
  for (int i = 0; i < 2; ++i) {
    const int c = t + 512 * i;
    if (c < 896) {
      const int row = c / 7, ch = c - row * 7;
      u32x4 w = *(const u32x4*)(st1 + row * 144 + ch * 16);
      u32x4 o;
#pragma unroll
      for (int e = 0; e < 4; ++e) o[e] = pack2(gelu_fast(lo2f(w[e])), gelu_fast(hi2f(w[e])));
      *(u32x4*)(z1 + (size_t)(m0 + row) * FP + c0 + ch * 8) = o;
    }
  }
  if (t < 128) {
    unsigned long long pv[16];
    for (int spin = 0; spin < 400000; ++spin) {
      bool ok = true;
#pragma unroll
      for (int j = 0; j < 16; ++j) pv[j] = __hip_atomic_load(pp + j * 128, __ATOMIC_RELAXED, __HIP_MEMORY_SCOPE_AGENT);
#pragma unroll
      for (int j = 0; j < 16; ++j)
        ok = ok && ((u32)pv[j] != 0xffffffffu) && ((u32)(pv[j] >> 32) != 0xffffffffu);
      if (ok) break;
      __builtin_amdgcn_s_sleep(1);
    }
    float ts = 0.f, tq = 0.f;
#pragma unroll
    for (int j = 0; j < 16; ++j) { ts += __uint_as_float((u32)pv[j]); tq += __uint_as_float((u32)(pv[j] >> 32)); }
    float mean = ts * (1.0f / FH);
    float var = fmaxf(tq * (1.0f / FH) - mean * mean, 0.f);
    sm[t] = rbf(mean);
    sm[128 + t] = 1.0f / rbf(sqrtf(rbf(rbf(var) + 1e-5f)));
  }
  lds_barrier();
  {
    float mm[8], ii[8];
#pragma unroll
    for (int e = 0; e < 8; ++e) { mm[e] = sm[sc * 8 + e]; ii[e] = sm[128 + sc * 8 + e]; }
#pragma unroll
    for (int i = 0; i < 2; ++i) {
      const int fl = (t >> 4) + 32 * i;
      if (fl < 56) {
        u32x4 o;
#pragma unroll
        for (int e = 0; e < 4; ++e)
          o[e] = pack2(rbf(lo2f(zo[i][e]) - mm[2 * e]) * ii[2 * e], rbf(hi2f(zo[i][e]) - mm[2 * e + 1]) * ii[2 * e + 1]);
        *(u32x4*)(z2nT + (size_t)(c0 + fl) * S_ + m0 + sc * 8) = o;
      }
    }
  }
}

constexpr int G2_NS = 5;
__global__ __launch_bounds__(512) void gemm2_kernel(const u16* __restrict__ w_sp, const u16* __restrict__ z2nT,
                                                    const u16* __restrict__ b_sp, const u16* __restrict__ z1,
                                                    u16* __restrict__ u) {
  __shared__ __attribute__((aligned(16))) char lds[G2_NS * 176 * 128];
  const int b = blockIdx.x, xcd = b & 7, idx = b >> 3;
  const int mt = (xcd >> 1) * 8 + (idx & 7);
  const int nt = (xcd & 1) * 4 + (idx >> 3);
  const int m0 = mt * 64, n0 = nt * 112;
  const int t = threadIdx.x, lane = t & 63, wave = t >> 6;
  const int fr = lane & 15, fq = lane >> 4;
  f32x4 acc[1][7];
#pragma unroll
  for (int j = 0; j < 7; ++j) acc[0][j] = (f32x4){0.f, 0.f, 0.f, 0.f};
  u32x4 zr[4];
  char* st = lds + (32 % G2_NS) * (176 * 128);
  float bias = 0.f;
  if (wave < 4) bias = bf2f(b_sp[m0 + wave * 16 + fr]);
  gemm_core<64, 112, G2_NS, 32, false, 2, 4>(w_sp + (size_t)m0 * S_, S_, z2nT + (size_t)n0 * S_, S_, lds, acc, [&] {
#pragma unroll
    for (int i = 0; i < 4; ++i) {
      int c = (t - 256) + 256 * i; if (c > 895) c = 895;
      const int row = c / 14, ch = c - row * 14;
      zr[i] = *(const u32x4*)(z1 + (size_t)(m0 + row) * FP + n0 + ch * 8);
    }
  });
  if (wave < 4) {
#pragma unroll
    for (int nj = 0; nj < 7; ++nj) {
      float g0 = rbf(acc[0][nj][0]) + bias, g1 = rbf(acc[0][nj][1]) + bias;
      float g2 = rbf(acc[0][nj][2]) + bias, g3 = rbf(acc[0][nj][3]) + bias;
      u32x2 o = {pack2(g0, g1), pack2(g2, g3)};
      *(u32x2*)(st + (wave * 16 + fr) * 240 + nj * 32 + fq * 8) = o;
    }
    lds_barrier();
  } else {
    lds_barrier();
#pragma unroll
    for (int i = 0; i < 4; ++i) {
      const int c = (t - 256) + 256 * i;
      if (c < 896) {
        const int row = c / 14, ch = c - row * 14;
        u32x4 g = *(const u32x4*)(st + row * 240 + ch * 16);
        u32x4 o;
#pragma unroll
        for (int e = 0; e < 4; ++e) o[e] = pack2(lo2f(zr[i][e]) * lo2f(g[e]), hi2f(zr[i][e]) * hi2f(g[e]));
        *(u32x4*)(u + (size_t)(m0 + row) * FP + n0 + ch * 8) = o;
      }
    }
  }
}

constexpr int G3_NS = 6;
__global__ __launch_bounds__(512) void gemm3_kernel(const u16* __restrict__ u, const u16* __restrict__ w_outT,
                                                    const u16* __restrict__ xres, u16* __restrict__ out) {
  __shared__ __attribute__((aligned(16))) char lds[G3_NS * 144 * 128];
  const int b = blockIdx.x, xcd = b & 7, idx = b >> 3;
  const int mt = (xcd >> 1) * 8 + (idx & 7);
  const int nt = (xcd & 1) * 4 + (idx >> 3);
  const int m0 = mt * 64, n0 = nt * 80;
  const int t = threadIdx.x, lane = t & 63, wave = t >> 6;
  const int fr = lane & 15, fq = lane >> 4;
  f32x4 acc[1][5];
#pragma unroll
  for (int j = 0; j < 5; ++j) acc[0][j] = (f32x4){0.f, 0.f, 0.f, 0.f};
  u32x4 xr[3];
  char* st = lds + (14 % G3_NS) * (144 * 128);
  gemm_core<64, 80, G3_NS, 14, false, 3, 3>(u + (size_t)m0 * FP, FP, w_outT + (size_t)n0 * FP, FP, lds, acc, [&] {
#pragma unroll
    for (int i = 0; i < 3; ++i) {
      int c = (t - 256) + 256 * i; if (c > 639) c = 639;
      const int row = c / 10, ch = c - row * 10;
      xr[i] = *(const u32x4*)(xres + (size_t)(m0 + row) * D_ + n0 + ch * 8);
    }
  });
  if (wave < 4) {
#pragma unroll
    for (int nj = 0; nj < 5; ++nj) {
      u32x2 o = {pack2(acc[0][nj][0], acc[0][nj][1]), pack2(acc[0][nj][2], acc[0][nj][3])};
      *(u32x2*)(st + (wave * 16 + fr) * 176 + nj * 32 + fq * 8) = o;
    }
    lds_barrier();
  } else {
    lds_barrier();
#pragma unroll
    for (int i = 0; i < 3; ++i) {
      const int c = (t - 256) + 256 * i;
      if (c < 640) {
        const int row = c / 10, ch = c - row * 10;
        u32x4 g = *(const u32x4*)(st + row * 176 + ch * 16);
        u32x4 o;
#pragma unroll
        for (int e = 0; e < 4; ++e) o[e] = pack2(lo2f(xr[i][e]) + lo2f(g[e]), hi2f(xr[i][e]) + hi2f(g[e]));
        *(u32x4*)(out + (size_t)(m0 + row) * D_ + n0 + ch * 8) = o;
      }
    }
  }
}

extern "C" void kernel_launch(void* const* d_in, const int* in_sizes, int n_in, void* d_out, int out_size,
                              void* d_ws, size_t ws_size, hipStream_t stream) {
  const u16* A_q = (const u16*)d_in[0];
  const u16* A_k = (const u16*)d_in[1];
  const u16* A_v = (const u16*)d_in[2];
  const u16* A_shift_k = (const u16*)d_in[3];
  const u16* A_ssm_k = (const u16*)d_in[4];
  const u16* A_ssm_D = (const u16*)d_in[5];
  const u16* B_w_in = (const u16*)d_in[7];
  const u16* B_w_sp = (const u16*)d_in[8];
  const u16* B_b_sp = (const u16*)d_in[9];
  const u16* B_w_out = (const u16*)d_in[10];
  u16* out = (u16*)d_out;

  char* ws = (char*)d_ws;
  size_t off = 0;
  auto take = [&](size_t bytes) { char* p = ws + off; off += (bytes + 255) & ~(size_t)255; return p; };
  u16* kvT = (u16*)take((size_t)D_ * S_ * 2);
  u16* YT = (u16*)take((size_t)D_ * S_ * 2);
  u16* w_inT = (u16*)take((size_t)2 * FP * D_ * 2);
  u16* w_outT = (u16*)take((size_t)D_ * FP * 2);
  u16* xa = (u16*)take((size_t)S_ * D_ * 2);
  u16* xb = (u16*)take((size_t)S_ * D_ * 2);
  u16* xn = (u16*)take((size_t)S_ * D_ * 2);
  u16* z1 = (u16*)take((size_t)S_ * FP * 2);
  u16* z2nT = (u16*)take((size_t)FP * S_ * 2);
  u16* u = (u16*)take((size_t)S_ * FP * 2);
  unsigned long long* part = (unsigned long long*)take((size_t)2 * 16 * 2048 * 8);

  pre_kernel<<<PRE_KV_BLOCKS + AUX_BLOCKS + PRE_TOUCH_BLOCKS, 256, 0, stream>>>(A_k, A_v, A_shift_k, A_ssm_k, B_w_in,
                                                                                 B_w_out, kvT, w_inT, w_outT,
                                                                                 (u32x4*)part);
  conv_kernel<<<D_, 256, 0, stream>>>(kvT, A_ssm_k, A_ssm_D, YT, B_w_sp, A_q);
  rowop_kernel<<<256, 512, 0, stream>>>(A_q, YT, xa, xn);
  gemm1_kernel<<<256, 512, 0, stream>>>(xn, w_inT, z1, z2nT, part);
  gemm2_kernel<<<256, 512, 0, stream>>>(B_w_sp, z2nT, B_b_sp, z1, u);
  gemm3_kernel<<<256, 512, 0, stream>>>(u, w_outT, xa, xb);
  rowop_kernel<<<256, 512, 0, stream>>>(xb, YT, xa, xn);
  gemm1_kernel<<<256, 512, 0, stream>>>(xn, w_inT, z1, z2nT, part + 16 * 2048);
  gemm2_kernel<<<256, 512, 0, stream>>>(B_w_sp, z2nT, B_b_sp, z1, u);
  gemm3_kernel<<<256, 512, 0, stream>>>(u, w_outT, xa, out);
}
```

```cpp
#include <hip/hip_runtime.h>
#include <hip/hip_bf16.h>
#include <stdint.h>

typedef unsigned short u16;
typedef unsigned int u32;
typedef __attribute__((ext_vector_type(8))) short bf16x8;
typedef __attribute__((ext_vector_type(4))) float f32x4;
typedef __attribute__((ext_vector_type(4))) unsigned int u32x4;
typedef __attribute__((ext_vector_type(2))) unsigned int u32x2;

#define DEV __device__ __forceinline__

constexpr int S_ = 2048, D_ = 640, FH = 864, FP = 896;

DEV float bf2f(u16 v) { return __uint_as_float(((u32)v) << 16); }
DEV float lo2f(u32 v) { return __uint_as_float(v << 16); }
DEV float hi2f(u32 v) { return __uint_as_float(v & 0xffff0000u); }
DEV float rbf(float f) { return (float)(__bf16)f; }
DEV u16 f2bf(float f) { __bf16 b = (__bf16)f; return __builtin_bit_cast(u16, b); }
DEV u32 pack2(float a, float b) { return (u32)f2bf(a) | ((u32)f2bf(b) << 16); }

template <int CTRL> DEV float dpp_add(float v) {
  return v + __int_as_float(__builtin_amdgcn_mov_dpp(__float_as_int(v), CTRL, 0xf, 0xf, true));
}
DEV float rdl(float v, int l) { return __int_as_float(__builtin_amdgcn_readlane(__float_as_int(v), l)); }
DEV float wave_sum(float v) {
  v = dpp_add<0xB1>(v);
  v = dpp_add<0x4E>(v);
  v = dpp_add<0x141>(v);
  v = dpp_add<0x140>(v);
  return (rdl(v, 0) + rdl(v, 16)) + (rdl(v, 32) + rdl(v, 48));
}

DEV float gelu_tanh(float x) {
  float y = 0.7978845608028654f * (x + 0.044715f * x * x * x);
  float e = __expf(-2.0f * y);
  return __fdividef(x, 1.0f + e);
}

constexpr int PRE_KV_BLOCKS = 320, PRE_TOUCH_BLOCKS = 64;
constexpr int AUX_WIN_BLOCKS = 270, AUX_WOUT_BLOCKS = 140, AUX_PAD_BLOCKS = 4;
constexpr int AUX_BLOCKS = AUX_WIN_BLOCKS + AUX_WOUT_BLOCKS + AUX_PAD_BLOCKS;

DEV void touch(const void* p, int n16, int blk, int nblk) {
  const u32x4* q = (const u32x4*)p;
  const int per = (n16 + nblk - 1) / nblk;
  const int lo = blk * per, hi = min(lo + per, n16);
  for (int i = lo + (int)threadIdx.x; i < hi; i += 256 * 8) {
    u32x4 w[8];
#pragma unroll
    for (int j = 0; j < 8; ++j) { int c = i + 256 * j; w[j] = q[c < hi ? c : i]; }
#pragma unroll
    for (int j = 0; j < 8; ++j) asm volatile("" ::"v"(w[j]));
  }
}

DEV void aux_work(int b, u16 (*tile)[72], const u16* __restrict__ w_in, const u16* __restrict__ w_out,
                  u16* __restrict__ w_inT, u16* __restrict__ w_outT) {
  const int t = threadIdx.x;
  if (b < AUX_WIN_BLOCKS + AUX_WOUT_BLOCKS) {
    const u16* src; u16* dst; int ld_src, ld_dst, r0, c0, rmax, drow0;
    if (b < AUX_WIN_BLOCKS) {
      int kt = b % 10, nt = b / 10;
      src = w_in; ld_src = 2 * FH; r0 = kt * 64; c0 = nt * 64; rmax = D_;
      dst = w_inT; ld_dst = D_; drow0 = 0;
    } else {
      int bb = b - AUX_WIN_BLOCKS;
      int kt = bb % 14, dt = bb / 14;
      src = w_out; ld_src = D_; r0 = kt * 64; c0 = dt * 64; rmax = FH;
      dst = w_outT; ld_dst = FP; drow0 = 1;
    }
#pragma unroll
    for (int h = 0; h < 2; ++h) {
      int idx = t + 256 * h;
      int r = idx >> 3, ch = idx & 7;
      u32x4 w = (u32x4){0u, 0u, 0u, 0u};
      if (r0 + r < rmax) w = *(const u32x4*)(src + (size_t)(r0 + r) * ld_src + c0 + ch * 8);
#pragma unroll
      for (int e = 0; e < 4; ++e) {
        tile[ch * 8 + 2 * e][r] = (u16)(w[e] & 0xffffu);
        tile[ch * 8 + 2 * e + 1][r] = (u16)(w[e] >> 16);
      }
    }
    __syncthreads();
#pragma unroll
    for (int h = 0; h < 2; ++h) {
      int idx = t + 256 * h;
      int cr = idx >> 3, ch = idx & 7;
      int n = c0 + cr;
      int drow = drow0 ? n : (n < FH ? n : n + 32);
      u32x4 o = *(const u32x4*)(&tile[cr][ch * 8]);
      *(u32x4*)(dst + (size_t)drow * ld_dst + r0 + ch * 8) = o;
    }
    return;
  }
  b -= AUX_WIN_BLOCKS + AUX_WOUT_BLOCKS;
  if (b < AUX_PAD_BLOCKS) {
    for (int i = b * 256 + t; i < 5120; i += AUX_PAD_BLOCKS * 256) {
      int row = i / 80, ch = i % 80;
      int rr = row < 32 ? FH + row : FP + FH + (row - 32);
      *(u32x4*)(w_inT + (size_t)rr * D_ + ch * 8) = (u32x4){0u, 0u, 0u, 0u};
    }
    return;
  }
}

__global__ __launch_bounds__(256) void pre_kernel(const u16* __restrict__ k, const u16* __restrict__ v,
                                                  const u16* __restrict__ shift_k, const u16* __restrict__ ssm_k,
                                                  const u16* __restrict__ w_in, const u16* __restrict__ w_out,
                                                  u16* __restrict__ kvT, u16* __restrict__ w_inT,
                                                  u16* __restrict__ w_outT, u32x4* __restrict__ part) {
  __shared__ u16 tile[64][72];
  const int t = threadIdx.x;
  int b = blockIdx.x;
  if (b < PRE_KV_BLOCKS) {
    const int s0 = (b & 31) * 64, d0 = (b >> 5) * 64;
    const int c8 = t & 7, r0 = t >> 3;
    const int d = d0 + c8 * 8;
    float sk[8][4];
#pragma unroll
    for (int j = 0; j < 4; ++j) {
      u32x4 w = *(const u32x4*)(shift_k + (d + 2 * j) * 4);
      sk[2 * j][0] = lo2f(w[0]); sk[2 * j][1] = hi2f(w[0]); sk[2 * j][2] = lo2f(w[1]); sk[2 * j][3] = hi2f(w[1]);
      sk[2 * j + 1][0] = lo2f(w[2]); sk[2 * j + 1][1] = hi2f(w[2]); sk[2 * j + 1][2] = lo2f(w[3]); sk[2 * j + 1][3] = hi2f(w[3]);
    }
#pragma unroll
    for (int h = 0; h < 2; ++h) {
      const int r = r0 + 32 * h;
      const int s = s0 + r;
      u32x4 kk[4];
#pragma unroll
      for (int j = 0; j < 4; ++j) {
        if (s - j >= 0) kk[j] = *(const u32x4*)(k + (size_t)(s - j) * D_ + d);
        else kk[j] = (u32x4){0u, 0u, 0u, 0u};
      }
      u32x4 vv = *(const u32x4*)(v + (size_t)s * D_ + d);
#pragma unroll
      for (int e = 0; e < 8; ++e) {
        float acc = 0.f;
#pragma unroll
        for (int tt = 0; tt < 4; ++tt) {
          u32 w = kk[3 - tt][e >> 1];
          float kf = (e & 1) ? hi2f(w) : lo2f(w);
          float p = rbf(kf * sk[e][3 - tt]);
          acc = rbf(acc + p);
        }
        u32 w = vv[e >> 1];
        float vf = (e & 1) ? hi2f(w) : lo2f(w);
        tile[c8 * 8 + e][r] = f2bf(acc * vf);
      }
    }
    __syncthreads();
#pragma unroll
    for (int h = 0; h < 2; ++h) {
      int idx = t + 256 * h;
      int dr = idx >> 3, ch = idx & 7;
      u32x4 o = *(const u32x4*)(&tile[dr][ch * 8]);
      *(u32x4*)(kvT + (size_t)(d0 + dr) * S_ + s0 + ch * 8) = o;
    }
    return;
  }
  b -= PRE_KV_BLOCKS;
  if (b < AUX_BLOCKS) { aux_work(b, tile, w_in, w_out, w_inT, w_outT); return; }
  b -= AUX_BLOCKS;
  part[b * 256 + t] = (u32x4){0xffffffffu, 0xffffffffu, 0xffffffffu, 0xffffffffu};
  touch(ssm_k, D_ * S_ / 8, b, PRE_TOUCH_BLOCKS);
}

constexpr int CV_E_BYTES = 2448 * 16;
constexpr int CV_X_BYTES = 32 * 272;

__global__ __launch_bounds__(256) void conv_kernel(const u16* __restrict__ kvT, const u16* __restrict__ ssm_k,
                                                   const u16* __restrict__ ssm_D, u16* __restrict__ YT,
                                                   const u16* __restrict__ w_sp, const u16* __restrict__ q) {
  __shared__ __attribute__((aligned(16))) char smem[CV_E_BYTES + CV_X_BYTES];
  char* Et = smem;
  char* Xp = smem + CV_E_BYTES;
  const int t = threadIdx.x, lane = t & 63;
  const int wave = __builtin_amdgcn_readfirstlane(t >> 6);
  const int d = blockIdx.x;
  const u16* h = ssm_k + (size_t)d * S_;
  const u16* x = kvT + (size_t)d * S_;
  {
    u32x4 w1 = *(const u32x4*)(h + 8 * t);
    u32x4 w0 = (u32x4){0u, 0u, 0u, 0u};
    if (t > 0) w0 = *(const u32x4*)(h + 8 * t - 8);
    u32 w[8] = {w0[0], w0[1], w0[2], w0[3], w1[0], w1[1], w1[2], w1[3]};
    char* base = Et + (144 + 9 * t) * 16;
#pragma unroll
    for (int r = 0; r < 8; ++r) {
      u32x4 g;
#pragma unroll
      for (int dd = 0; dd < 4; ++dd) {
        if ((r & 1) == 0) {
          int p = (8 + r - 2 * dd) >> 1;
          g[dd] = (w[p] & 0xffffu) | (w[p - 1] & 0xffff0000u);
        } else {
          int p = (8 + r - 2 * dd - 1) >> 1;
          g[dd] = (w[p] >> 16) | (w[p] << 16);
        }
      }
      *(u32x4*)(base + r * 16) = g;
    }
    if (t < 144) *(u32x4*)(Et + t * 16) = (u32x4){0u, 0u, 0u, 0u};
    u32x4 xv = *(const u32x4*)(x + 8 * t);
    *(u32x4*)(Xp + (16 + (t >> 4)) * 272 + (t & 15) * 16) = xv;
    for (int i = t; i < 16 * 17; i += 256) *(u32x4*)(Xp + i * 16) = (u32x4){0u, 0u, 0u, 0u};
  }
  __syncthreads();
  u32x4 pf[5];
  {
    const int base = d * 256 + t;
#pragma unroll
    for (int j = 0; j < 4; ++j) {
      int c = base + j * (D_ * 256); if (c >= S_ * S_ / 8) c = base;
      pf[j] = ((const u32x4*)w_sp)[c];
    }
    pf[4] = ((const u32x4*)q)[base];
  }
  const int p = wave & 1, hk = wave >> 1;
  const int m = lane & 15, kg = lane >> 4;
  const char* abase = Et + (m + (m >> 3) - 9 * kg) * 16 + 288 * p + 2304;
  const char* bbase = Xp + (m + 16) * 272 + kg * 16;
  f32x4 acc[4];
#pragma unroll
  for (int i = 0; i < 4; ++i) acc[i] = (f32x4){0.f, 0.f, 0.f, 0.f};
  bf16x8 F[4];
  const int D0 = 8 * hk;
  F[1] = *(const bf16x8*)(abase + 576 * (4 * D0 - 3));
  F[2] = *(const bf16x8*)(abase + 576 * (4 * D0 - 2));
  F[3] = *(const bf16x8*)(abase + 576 * (4 * D0 - 1));
  for (int dl = 0; dl < 8; ++dl) {
    const int Dl = D0 + dl;
    const char* ab = abase + 576 * 4 * Dl;
    const char* bb = bbase - 272 * Dl;
#pragma unroll
    for (int qq = 0; qq < 4; ++qq) {
      const int q = 3 - qq;
      F[qq] = *(const bf16x8*)(ab + 576 * qq);
      bf16x8 B = *(const bf16x8*)(bb + 64 * q);
#pragma unroll
      for (int i = 0; i < 4; ++i) {
        acc[i] = __builtin_amdgcn_mfma_f32_16x16x32_bf16(F[(i - q) & 3], B, acc[i], 0, 0, 0);
      }
    }
  }
  __syncthreads();
  float* yp = (float*)Et;
  u16* orow = (u16*)(Et + 8704);
  const int a = m, g = kg;
  if (hk == 1) {
#pragma unroll
    for (int i = 0; i < 4; ++i) {
      int mi = p + 2 * i;
      *(f32x4*)(yp + a * 132 + 16 * mi + 4 * g) = acc[i];
    }
  }
  __syncthreads();
  if (hk == 0) {
    const float Dv = bf2f(ssm_D[d]);
#pragma unroll
    for (int i = 0; i < 4; ++i) {
      int mi = p + 2 * i;
      f32x4 o = *(const f32x4*)(yp + a * 132 + 16 * mi + 4 * g);
      u32x2 xv = *(const u32x2*)(Xp + (a + 16) * 272 + (16 * mi + 4 * g) * 2);
      float xs[4] = {lo2f(xv[0]), hi2f(xv[0]), lo2f(xv[1]), hi2f(xv[1])};
      float res[4];
#pragma unroll
      for (int r = 0; r < 4; ++r) {
        float y = rbf(acc[i][r] + o[r]);
        float kd = rbf(xs[r] * Dv);
        res[r] = y + kd;
      }
      u32x2 ov = {pack2(res[0], res[1]), pack2(res[2], res[3])};
      *(u32x2*)(orow + 128 * a + 16 * mi + 4 * g) = ov;
    }
  }
  __syncthreads();
  *(u32x4*)(YT + (size_t)d * S_ + 8 * t) = *(const u32x4*)(orow + 8 * t);
#pragma unroll
  for (int j = 0; j < 5; ++j) asm volatile("" ::"v"(pf[j]));
}

DEV void ln10(const float (&v)[10], float (&o)[10]) {
  float s = 0.f;
#pragma unroll
  for (int i = 0; i < 10; ++i) s += v[i];
  s = wave_sum(s);
  float mean = s * (1.0f / 640.0f);
  float q = 0.f;
#pragma unroll
  for (int i = 0; i < 10; ++i) { float dd = v[i] - mean; q += dd * dd; }
  q = wave_sum(q);
  float var = rbf(q * (1.0f / 640.0f));
  float mb = rbf(mean);
  float den = rbf(sqrtf(rbf(var + 1e-5f)));
  float inv = 1.0f / den;
#pragma unroll
  for (int i = 0; i < 10; ++i) o[i] = rbf(rbf(v[i] - mb) * inv);
}

__global__ __launch_bounds__(512) void rowop_kernel(const u16* __restrict__ xin, const u16* __restrict__ YT,
                                                    u16* __restrict__ xo, u16* __restrict__ xn) {
  __shared__ u16 yt[8][648];
  const int t = threadIdx.x, lane = t & 63, r = t >> 6;
  const int s0 = ((blockIdx.x & 7) * 32 + (blockIdx.x >> 3)) * 8;
  for (int d = t; d < D_; d += 512) {
    u32x4 w = *(const u32x4*)(YT + (size_t)d * S_ + s0);
#pragma unroll
    for (int e = 0; e < 4; ++e) {
      yt[2 * e][d] = (u16)(w[e] & 0xffffu);
      yt[2 * e + 1][d] = (u16)(w[e] >> 16);
    }
  }
  float v[10];
  const u16* xr = xin + (size_t)(s0 + r) * D_;
#pragma unroll
  for (int i = 0; i < 5; ++i) {
    u32 w = *(const u32*)(xr + 2 * lane + 128 * i);
    v[2 * i] = lo2f(w); v[2 * i + 1] = hi2f(w);
  }
  float a[10];
  ln10(v, a);
  __syncthreads();
  float x1[10];
#pragma unroll
  for (int i = 0; i < 5; ++i) {
    u32 w = *(const u32*)(&yt[r][2 * lane + 128 * i]);
    x1[2 * i] = rbf(v[2 * i] + rbf(a[2 * i] * lo2f(w)));
    x1[2 * i + 1] = rbf(v[2 * i + 1] + rbf(a[2 * i + 1] * hi2f(w)));
  }
  float n1[10];
  ln10(x1, n1);
  u16* xor_ = xo + (size_t)(s0 + r) * D_;
  u16* xnr = xn + (size_t)(s0 + r) * D_;
#pragma unroll
  for (int i = 0; i < 5; ++i) {
    *(u32*)(xor_ + 2 * lane + 128 * i) = pack2(x1[2 * i], x1[2 * i + 1]);
    *(u32*)(xnr + 2 * lane + 128 * i) = pack2(n1[2 * i], n1[2 * i + 1]);
  }
}

template <int N> DEV void wait_vm() {
  asm volatile("s_waitcnt vmcnt(%0)" ::"n"(N) : "memory");
}

template <int BM, int BN, int NS, int NK, bool SWAP, int KID, int EXTRA, class PRE>
DEV void gemm_core(const u16* __restrict__ A, int lda, const u16* __restrict__ Bt, int ldb, char* lds,
                   f32x4 (&acc)[BM / 64][BN / 16], PRE&& pre) {
  constexpr int T = (BM + BN) / 8;
  constexpr int NI = (T + 3) / 4;
  constexpr int REM = T % 4;
  constexpr int STAGE = (BM + BN) * 128;
  constexpr int MI = BM / 64, NJ = BN / 16;
  const int t = threadIdx.x, lane = t & 63;
  const int wave = __builtin_amdgcn_readfirstlane(t >> 6);
  if (wave >= 4) {
    const int lw = wave - 4;
    const u16* src[NI];
#pragma unroll
    for (int j = 0; j < NI; ++j) {
      int i = lw + 4 * j;
      if (i >= T) i = T - 1;
      int row = 8 * i + (lane >> 3);
      int cg = (lane & 7) ^ (row & 7);
      src[j] = (8 * i < BM) ? (A + (size_t)row * lda + cg * 8) : (Bt + (size_t)(row - BM) * ldb + cg * 8);
    }
    const bool full = (REM == 0) || (lw < REM);
    auto issue = [&](int kt) {
      char* dst = lds + (kt % NS) * STAGE + lw * 1024;
#pragma unroll
      for (int j = 0; j < NI; ++j) {
        if (j < NI - 1 || full)
          __builtin_amdgcn_global_load_lds((const u32*)(src[j] + kt * 64), (u32*)(dst + j * 4096), 16, 0, 0);
      }
    };
    auto waitfor = [&](int rem) {
      if (rem <= 0) wait_vm<0>();
      else if (rem == 1) { if (full) wait_vm<NI>(); else wait_vm<NI - 1>(); }
      else if (rem == 2) { if (full) wait_vm<2 * NI>(); else wait_vm<2 * (NI - 1)>(); }
      else if (rem == 3) { if (full) wait_vm<3 * NI>(); else wait_vm<3 * (NI - 1)>(); }
      else if (rem == 4) { if (full) wait_vm<4 * NI>(); else wait_vm<4 * (NI - 1)>(); }
      else if (rem == 5) { if (full) wait_vm<5 * NI>(); else wait_vm<5 * (NI - 1)>(); }
      else wait_vm<0>();
    };
    auto waitforx = [&](int rem) {
      if (rem <= 0) wait_vm<EXTRA>();
      else if (rem == 1) { if (full) wait_vm<EXTRA + NI>(); else wait_vm<EXTRA + NI - 1>(); }
      else if (rem == 2) { if (full) wait_vm<EXTRA + 2 * NI>(); else wait_vm<EXTRA + 2 * (NI - 1)>(); }
      else if (rem == 3) { if (full) wait_vm<EXTRA + 3 * NI>(); else wait_vm<EXTRA + 3 * (NI - 1)>(); }
      else if (rem == 4) { if (full) wait_vm<EXTRA + 4 * NI>(); else wait_vm<EXTRA + 4 * (NI - 1)>(); }
      else if (rem == 5) { if (full) wait_vm<EXTRA + 5 * NI>(); else wait_vm<EXTRA + 5 * (NI - 1)>(); }
      else wait_vm<0>();
    };
    static_assert(NS - 2 <= 5, "");
#pragma unroll
    for (int s = 0; s < NS - 1; ++s) issue(s);
    constexpr int MAINK = NK - (NS - 1);
    for (int kt = 0; kt < MAINK; ++kt) {
      waitfor(NS - 2);
      __builtin_amdgcn_s_barrier();
      issue(kt + NS - 1);
    }
    pre();
#pragma unroll
    for (int kt = MAINK; kt < NK; ++kt) {
      waitforx(NK - 1 - kt);
      __builtin_amdgcn_s_barrier();
    }
    return;
  }
  const int fr = lane & 15, fq = lane >> 4;
  int aoff[MI], boff[NJ];
#pragma unroll
  for (int mi = 0; mi < MI; ++mi) aoff[mi] = (wave * (BM / 4) + 16 * mi + fr) * 128;
#pragma unroll
  for (int nj = 0; nj < NJ; ++nj) boff[nj] = (BM + 16 * nj + fr) * 128;
  const int sw = fr & 7;
  bf16x8 fa[2][2][MI], fb[2][2][NJ];
  auto rd = [&](int kt, bf16x8 (&a)[2][MI], bf16x8 (&bb)[2][NJ]) {
    const char* base = lds + (kt % NS) * STAGE;
#pragma unroll
    for (int kk = 0; kk < 2; ++kk) {
      const int ch = ((4 * kk + fq) ^ sw) * 16;
#pragma unroll
      for (int mi = 0; mi < MI; ++mi) a[kk][mi] = *(const bf16x8*)(base + aoff[mi] + ch);
#pragma unroll
      for (int nj = 0; nj < NJ; ++nj) bb[kk][nj] = *(const bf16x8*)(base + boff[nj] + ch);
    }
  };
  auto mma = [&](bf16x8 (&a)[2][MI], bf16x8 (&bb)[2][NJ]) {
#pragma unroll
    for (int kk = 0; kk < 2; ++kk)
#pragma unroll
      for (int mi = 0; mi < MI; ++mi)
#pragma unroll
        for (int nj = 0; nj < NJ; ++nj) {
          if (SWAP) acc[mi][nj] = __builtin_amdgcn_mfma_f32_16x16x32_bf16(bb[kk][nj], a[kk][mi], acc[mi][nj], 0, 0, 0);
          else acc[mi][nj] = __builtin_amdgcn_mfma_f32_16x16x32_bf16(a[kk][mi], bb[kk][nj], acc[mi][nj], 0, 0, 0);
        }
  };
  static_assert(NK % 2 == 0, "");
  auto ilv = [&]() {
    constexpr int NR = 2 * (MI + NJ), NM = 2 * MI * NJ;
    if constexpr (NM >= NR) {
      constexpr int PER = NM / NR;
#pragma unroll
      for (int i = 0; i < NR; ++i) {
        __builtin_amdgcn_sched_group_barrier(0x100, 1, 0);
        __builtin_amdgcn_sched_group_barrier(0x008, PER, 0);
      }
      __builtin_amdgcn_sched_group_barrier(0x008, NM - PER * NR, 0);
    } else {
#pragma unroll
      for (int i = 0; i < NM; ++i) {
        __builtin_amdgcn_sched_group_barrier(0x100, 1, 0);
        __builtin_amdgcn_sched_group_barrier(0x008, 1, 0);
      }
      __builtin_amdgcn_sched_group_barrier(0x100, NR - NM, 0);
    }
  };
  auto lgk0 = [&]() { __builtin_amdgcn_sched_barrier(0); asm volatile("s_waitcnt lgkmcnt(0)" ::: "memory"); };
  __builtin_amdgcn_s_barrier();
  rd(0, fa[0], fb[0]);
  __builtin_amdgcn_sched_barrier(0);
  lgk0();
  __builtin_amdgcn_s_barrier();
  rd(1, fa[1], fb[1]);
  __builtin_amdgcn_sched_barrier(0);
  mma(fa[0], fb[0]);
  for (int kt = 2; kt < NK; kt += 2) {
    lgk0();
    __builtin_amdgcn_s_barrier();
    rd(kt, fa[0], fb[0]);
    mma(fa[1], fb[1]);
    ilv();
    lgk0();
    __builtin_amdgcn_s_barrier();
    rd(kt + 1, fa[1], fb[1]);
    mma(fa[0], fb[0]);
    ilv();
  }
  mma(fa[1], fb[1]);
}

DEV float gelu_fast(float x) {
  const float c1 = -2.0f * 0.7978845608028654f * 1.4426950408889634f;
  const float c2 = c1 * 0.044715f;
  float e = __builtin_amdgcn_exp2f(x * __builtin_fmaf(x * x, c2, c1));
  return x * __builtin_amdgcn_rcpf(1.0f + e);
}
typedef __attribute__((ext_vector_type(2))) float f32x2;
typedef __attribute__((ext_vector_type(2))) __bf16 bf16x2;
DEV u32 pack2v(f32x2 v) { return __builtin_bit_cast(u32, __builtin_convertvector(v, bf16x2)); }
DEV f32x2 unpk(u32 w) { return (f32x2){lo2f(w), hi2f(w)}; }
DEV u32 gelu2(u32 w) {
  const float c1 = -2.0f * 0.7978845608028654f * 1.4426950408889634f;
  const float c2 = c1 * 0.044715f;
  f32x2 x = {lo2f(w), hi2f(w)};
  f32x2 a = x * __builtin_elementwise_fma(x * x, (f32x2){c2, c2}, (f32x2){c1, c1});
  f32x2 d = (f32x2){__builtin_amdgcn_exp2f(a[0]), __builtin_amdgcn_exp2f(a[1])} + (f32x2){1.0f, 1.0f};
  f32x2 y = x * (f32x2){__builtin_amdgcn_rcpf(d[0]), __builtin_amdgcn_rcpf(d[1])};
  return pack2v(y);
}
DEV void lds_barrier() { asm volatile("s_waitcnt lgkmcnt(0)" ::: "memory"); __builtin_amdgcn_s_barrier(); asm volatile("" ::: "memory"); }

constexpr int G1_NS = 4;
__global__ __launch_bounds__(512) void gemm1_kernel(const u16* __restrict__ xn, const u16* __restrict__ w_inT,
                                                    u16* __restrict__ z1, u16* __restrict__ z2nT,
                                                    unsigned long long* __restrict__ part) {
  __shared__ __attribute__((aligned(16))) char lds[G1_NS * 240 * 128];
  const int b = blockIdx.x, xcd = b & 7, idx = b >> 3;
  const int mt = (xcd >> 1) * 4 + (idx & 3);
  const int nt = (xcd & 1) * 8 + (idx >> 2);
  const int m0 = mt * 128, n0 = nt * 112;
  const int t = threadIdx.x, lane = t & 63, wave = t >> 6;
  const int fr = lane & 15, fq = lane >> 4;
  f32x4 acc[2][7];
#pragma unroll
  for (int i = 0; i < 2; ++i)
#pragma unroll
    for (int j = 0; j < 7; ++j) acc[i][j] = (f32x4){0.f, 0.f, 0.f, 0.f};
  char* st = lds + (10 % G1_NS) * (240 * 128);
  if (nt < 8) {
    gemm_core<128, 112, G1_NS, 10, true, 1, 0>(xn + (size_t)m0 * D_, D_, w_inT + (size_t)n0 * D_, D_, lds, acc, [] {});
    if (wave < 4) {
#pragma unroll
      for (int mi = 0; mi < 2; ++mi)
#pragma unroll
        for (int nj = 0; nj < 7; ++nj) {
          u32x2 o = {pack2(acc[mi][nj][0], acc[mi][nj][1]), pack2(acc[mi][nj][2], acc[mi][nj][3])};
          *(u32x2*)(st + (wave * 32 + 16 * mi + fr) * 240 + nj * 32 + fq * 8) = o;
        }
    }
    lds_barrier();
#pragma unroll
    for (int i = 0; i < 4; ++i) {
      const int c = t + 512 * i;
      if (c < 1792) {
        const int row = c / 14, ch = c - row * 14;
        u32x4 w = *(const u32x4*)(st + row * 240 + ch * 16);
        u32x4 o;
#pragma unroll
        for (int e = 0; e < 4; ++e) o[e] = gelu2(w[e]);
        *(u32x4*)(z1 + (size_t)(m0 + row) * FP + n0 + ch * 8) = o;
      }
    }
  } else {
    gemm_core<128, 112, G1_NS, 10, false, 1, 0>(xn + (size_t)m0 * D_, D_, w_inT + (size_t)n0 * D_, D_, lds, acc, [] {});
    const int nt2 = nt - 8;
    const int f0 = nt2 * 112;
    if (wave < 4) {
#pragma unroll
      for (int mi = 0; mi < 2; ++mi)
#pragma unroll
        for (int nj = 0; nj < 7; ++nj) {
          u32x2 o = {pack2(acc[mi][nj][0], acc[mi][nj][1]), pack2(acc[mi][nj][2], acc[mi][nj][3])};
          *(u32x2*)(st + (16 * nj + fr) * 272 + (wave * 32 + 16 * mi + 4 * fq) * 2) = o;
        }
    }
    lds_barrier();
    f32x2 sum2[4], sq2[4];
#pragma unroll
    for (int e = 0; e < 4; ++e) { sum2[e] = (f32x2){0.f, 0.f}; sq2[e] = (f32x2){0.f, 0.f}; }
    const int sc = t & 15;
    u32x4 zo[4];
#pragma unroll
    for (int i = 0; i < 4; ++i) {
      const int fl = (t >> 4) + 32 * i;
      zo[i] = (u32x4){0u, 0u, 0u, 0u};
      if (fl < 112) {
        u32x4 w = *(const u32x4*)(st + fl * 272 + sc * 16);
#pragma unroll
        for (int e = 0; e < 4; ++e) zo[i][e] = gelu2(w[e]);
        if (f0 + fl < FH) {
#pragma unroll
          for (int e = 0; e < 4; ++e) {
            f32x2 a = {lo2f(zo[i][e]), hi2f(zo[i][e])};
            sum2[e] += a;
            sq2[e] = __builtin_elementwise_fma(a, a, sq2[e]);
          }
        }
      }
    }
    float sum[8], sq[8];
#pragma unroll
    for (int e = 0; e < 4; ++e) { sum[2 * e] = sum2[e][0]; sum[2 * e + 1] = sum2[e][1]; sq[2 * e] = sq2[e][0]; sq[2 * e + 1] = sq2[e][1]; }
#pragma unroll
    for (int e = 0; e < 8; ++e) {
      sum[e] += __shfl_xor(sum[e], 16); sum[e] += __shfl_xor(sum[e], 32);
      sq[e] += __shfl_xor(sq[e], 16); sq[e] += __shfl_xor(sq[e], 32);
    }
    float2* red = (float2*)(lds + (11 % G1_NS) * (240 * 128));
    float* sm = (float*)(red + 8 * 128);
    if (lane < 16) {
#pragma unroll
      for (int e = 0; e < 8; ++e) red[wave * 128 + sc * 8 + e] = make_float2(sum[e], sq[e]);
    }
    lds_barrier();
    if (t < 128) {
      float a = 0.f, q = 0.f;
#pragma unroll
      for (int w = 0; w < 8; ++w) { float2 v = red[w * 128 + t]; a += v.x; q += v.y; }
      u32 ab = __float_as_uint(a), qb = __float_as_uint(q);
      if (ab == 0xffffffffu) ab = 0x7fc00000u;
      if (qb == 0xffffffffu) qb = 0x7fc00000u;
      unsigned long long* pp = part + (size_t)mt * 1024 + t;
      __hip_atomic_store(pp + nt2 * 128, (unsigned long long)ab | ((unsigned long long)qb << 32), __ATOMIC_RELAXED,
                         __HIP_MEMORY_SCOPE_AGENT);
      unsigned long long pv[8];
      for (int spin = 0; spin < 400000; ++spin) {
        bool ok = true;
#pragma unroll
        for (int j = 0; j < 8; ++j) pv[j] = __hip_atomic_load(pp + j * 128, __ATOMIC_RELAXED, __HIP_MEMORY_SCOPE_AGENT);
#pragma unroll
        for (int j = 0; j < 8; ++j)
          ok = ok && ((u32)pv[j] != 0xffffffffu) && ((u32)(pv[j] >> 32) != 0xffffffffu);
        if (ok) break;
        __builtin_amdgcn_s_sleep(1);
      }
      float ts = 0.f, tq = 0.f;
#pragma unroll
      for (int j = 0; j < 8; ++j) { ts += __uint_as_float((u32)pv[j]); tq += __uint_as_float((u32)(pv[j] >> 32)); }
      float mean = ts * (1.0f / FH);
      float var = fmaxf(tq * (1.0f / FH) - mean * mean, 0.f);
      sm[t] = rbf(mean);
      sm[128 + t] = 1.0f / rbf(sqrtf(rbf(rbf(var) + 1e-5f)));
    }
    lds_barrier();
    {
      float mm[8], ii[8];
#pragma unroll
      for (int e = 0; e < 8; ++e) { mm[e] = sm[sc * 8 + e]; ii[e] = sm[128 + sc * 8 + e]; }
#pragma unroll
      for (int i = 0; i < 4; ++i) {
        const int fl = (t >> 4) + 32 * i;
        if (fl < 112) {
          u32x4 o;
#pragma unroll
          for (int e = 0; e < 4; ++e)
            o[e] = pack2v(unpk(pack2v(unpk(zo[i][e]) - (f32x2){mm[2 * e], mm[2 * e + 1]})) * (f32x2){ii[2 * e], ii[2 * e + 1]});
          *(u32x4*)(z2nT + (size_t)(f0 + fl) * S_ + m0 + sc * 8) = o;
        }
      }
    }
  }
}

constexpr int G2_NS = 5;
__global__ __launch_bounds__(512) void gemm2_kernel(const u16* __restrict__ w_sp, const u16* __restrict__ z2nT,
                                                    const u16* __restrict__ b_sp, const u16* __restrict__ z1,
                                                    u16* __restrict__ u) {
  __shared__ __attribute__((aligned(16))) char lds[G2_NS * 176 * 128];
  const int b = blockIdx.x, xcd = b & 7, idx = b >> 3;
  const int mt = (xcd >> 1) * 8 + (idx & 7);
  const int nt = (xcd & 1) * 4 + (idx >> 3);
  const int m0 = mt * 64, n0 = nt * 112;
  const int t = threadIdx.x, lane = t & 63, wave = t >> 6;
  const int fr = lane & 15, fq = lane >> 4;
  f32x4 acc[1][7];
#pragma unroll
  for (int j = 0; j < 7; ++j) acc[0][j] = (f32x4){0.f, 0.f, 0.f, 0.f};
  u32x4 zr[4];
  char* st = lds + (32 % G2_NS) * (176 * 128);
  float bias = 0.f;
  if (wave < 4) bias = bf2f(b_sp[m0 + wave * 16 + fr]);
  gemm_core<64, 112, G2_NS, 32, true, 2, 4>(w_sp + (size_t)m0 * S_, S_, z2nT + (size_t)n0 * S_, S_, lds, acc, [&] {
#pragma unroll
    for (int i = 0; i < 4; ++i) {
      int c = (t - 256) + 256 * i; if (c > 895) c = 895;
      const int row = c / 14, ch = c - row * 14;
      zr[i] = *(const u32x4*)(z1 + (size_t)(m0 + row) * FP + n0 + ch * 8);
    }
  });
  if (wave < 4) {
#pragma unroll
    for (int nj = 0; nj < 7; ++nj) {
      float g0 = rbf(acc[0][nj][0]) + bias, g1 = rbf(acc[0][nj][1]) + bias;
      float g2 = rbf(acc[0][nj][2]) + bias, g3 = rbf(acc[0][nj][3]) + bias;
      u32x2 o = {pack2(g0, g1), pack2(g2, g3)};
      *(u32x2*)(st + (wave * 16 + fr) * 240 + nj * 32 + fq * 8) = o;
    }
    lds_barrier();
  } else {
    lds_barrier();
#pragma unroll
    for (int i = 0; i < 4; ++i) {
      const int c = (t - 256) + 256 * i;
      if (c < 896) {
        const int row = c / 14, ch = c - row * 14;
        u32x4 g = *(const u32x4*)(st + row * 240 + ch * 16);
        u32x4 o;
#pragma unroll
        for (int e = 0; e < 4; ++e) o[e] = pack2(lo2f(zr[i][e]) * lo2f(g[e]), hi2f(zr[i][e]) * hi2f(g[e]));
        *(u32x4*)(u + (size_t)(m0 + row) * FP + n0 + ch * 8) = o;
      }
    }
  }
}

constexpr int G3_NS = 6;
__global__ __launch_bounds__(512) void gemm3_kernel(const u16* __restrict__ u, const u16* __restrict__ w_outT,
                                                    const u16* __restrict__ xres, u16* __restrict__ out) {
  __shared__ __attribute__((aligned(16))) char lds[G3_NS * 144 * 128];
  const int b = blockIdx.x, xcd = b & 7, idx = b >> 3;
  const int mt = (xcd >> 1) * 8 + (idx & 7);
  const int nt = (xcd & 1) * 4 + (idx >> 3);
  const int m0 = mt * 64, n0 = nt * 80;
  const int t = threadIdx.x, lane = t & 63, wave = t >> 6;
  const int fr = lane & 15, fq = lane >> 4;
  f32x4 acc[1][5];
#pragma unroll
  for (int j = 0; j < 5; ++j) acc[0][j] = (f32x4){0.f, 0.f, 0.f, 0.f};
  u32x4 xr[3];
  char* st = lds + (14 % G3_NS) * (144 * 128);
  gemm_core<64, 80, G3_NS, 14, true, 3, 3>(u + (size_t)m0 * FP, FP, w_outT + (size_t)n0 * FP, FP, lds, acc, [&] {
#pragma unroll
    for (int i = 0; i < 3; ++i) {
      int c = (t - 256) + 256 * i; if (c > 639) c = 639;
      const int row = c / 10, ch = c - row * 10;
      xr[i] = *(const u32x4*)(xres + (size_t)(m0 + row) * D_ + n0 + ch * 8);
    }
  });
  if (wave < 4) {
#pragma unroll
    for (int nj = 0; nj < 5; ++nj) {
      u32x2 o = {pack2(acc[0][nj][0], acc[0][nj][1]), pack2(acc[0][nj][2], acc[0][nj][3])};
      *(u32x2*)(st + (wave * 16 + fr) * 176 + nj * 32 + fq * 8) = o;
    }
    lds_barrier();
  } else {
    lds_barrier();
#pragma unroll
    for (int i = 0; i < 3; ++i) {
      const int c = (t - 256) + 256 * i;
      if (c < 640) {
        const int row = c / 10, ch = c - row * 10;
        u32x4 g = *(const u32x4*)(st + row * 176 + ch * 16);
        u32x4 o;
#pragma unroll
        for (int e = 0; e < 4; ++e) o[e] = pack2(lo2f(xr[i][e]) + lo2f(g[e]), hi2f(xr[i][e]) + hi2f(g[e]));
        *(u32x4*)(out + (size_t)(m0 + row) * D_ + n0 + ch * 8) = o;
      }
    }
  }
}

extern "C" void kernel_launch(void* const* d_in, const int* in_sizes, int n_in, void* d_out, int out_size,
                              void* d_ws, size_t ws_size, hipStream_t stream) {
  const u16* A_q = (const u16*)d_in[0];
  const u16* A_k = (const u16*)d_in[1];
  const u16* A_v = (const u16*)d_in[2];
  const u16* A_shift_k = (const u16*)d_in[3];
  const u16* A_ssm_k = (const u16*)d_in[4];
  const u16* A_ssm_D = (const u16*)d_in[5];
  const u16* B_w_in = (const u16*)d_in[7];
  const u16* B_w_sp = (const u16*)d_in[8];
  const u16* B_b_sp = (const u16*)d_in[9];
  const u16* B_w_out = (const u16*)d_in[10];
  u16* out = (u16*)d_out;

  char* ws = (char*)d_ws;
  size_t off = 0;
  auto take = [&](size_t bytes) { char* p = ws + off; off += (bytes + 255) & ~(size_t)255; return p; };
  u16* kvT = (u16*)take((size_t)D_ * S_ * 2);
  u16* YT = (u16*)take((size_t)D_ * S_ * 2);
  u16* w_inT = (u16*)take((size_t)2 * FP * D_ * 2);
  u16* w_outT = (u16*)take((size_t)D_ * FP * 2);
  u16* xa = (u16*)take((size_t)S_ * D_ * 2);
  u16* xb = (u16*)take((size_t)S_ * D_ * 2);
  u16* xn = (u16*)take((size_t)S_ * D_ * 2);
  u16* z1 = (u16*)take((size_t)S_ * FP * 2);
  u16* z2nT = (u16*)take((size_t)FP * S_ * 2);
  u16* u = (u16*)take((size_t)S_ * FP * 2);
  unsigned long long* part = (unsigned long long*)take((size_t)2 * 16 * 1024 * 8);

  pre_kernel<<<PRE_KV_BLOCKS + AUX_BLOCKS + PRE_TOUCH_BLOCKS, 256, 0, stream>>>(A_k, A_v, A_shift_k, A_ssm_k, B_w_in,
                                                                                 B_w_out, kvT, w_inT, w_outT,
                                                                                 (u32x4*)part);
  conv_kernel<<<D_, 256, 0, stream>>>(kvT, A_ssm_k, A_ssm_D, YT, B_w_sp, A_q);
  rowop_kernel<<<256, 512, 0, stream>>>(A_q, YT, xa, xn);
  gemm1_kernel<<<256, 512, 0, stream>>>(xn, w_inT, z1, z2nT, part);
  gemm2_kernel<<<256, 512, 0, stream>>>(B_w_sp, z2nT, B_b_sp, z1, u);
  gemm3_kernel<<<256, 512, 0, stream>>>(u, w_outT, xa, xb);
  rowop_kernel<<<256, 512, 0, stream>>>(xb, YT, xa, xn);
  gemm1_kernel<<<256, 512, 0, stream>>>(xn, w_inT, z1, z2nT, part + 16 * 1024);
  gemm2_kernel<<<256, 512, 0, stream>>>(B_w_sp, z2nT, B_b_sp, z1, u);
  gemm3_kernel<<<256, 512, 0, stream>>>(u, w_outT, xa, out);
}
```
